```python
import torch

def _softmax(x, axis=-1):
    return torch.softmax(x, dim=axis)

def _gelu(x):
    return torch.nn.functional.gelu(x, approximate='tanh')

def _silu(x):
    return torch.nn.functional.silu(x)

def _sigmoid(x):
    return torch.sigmoid(x)

def _layernorm(x, axis=-1, eps=1e-05):
    m = x.mean(dim=axis, keepdim=True)
    v = x.var(dim=axis, keepdim=True, unbiased=False)
    return (x - m) / torch.sqrt(v + eps)

def _scatter_add(tgt, idx, src, axis=0):
    return tgt.clone().index_add_(axis, idx, src)

def _segment_sum(src, seg, n):
    out = torch.zeros((n,) + src.shape[1:], dtype=src.dtype)
    return out.index_add_(0, seg, src)

def _pad(x, pw):
    flat = tuple((v for pair in reversed(pw) for v in pair))
    return torch.nn.functional.pad(x, flat)
kernel_name = 'adaLN_cond__ssm_conv_s4__dense_activation_squared_relu__prompt'

def setup_inputs():

    _w = lambda a: (lambda t: t.to(torch.bfloat16) if t.is_floating_point() else t)(torch.as_tensor(a))

    def _A_setup():
        B, L, D, N = (1, 2048, 512, 128)
        A_re = -(lambda _x: torch.exp(torch.as_tensor(_x)))((lambda *s: torch.randn(*s, dtype=torch.bfloat16) * 12.8)(D, N))
        A_im = torch.pi * (lambda *s: torch.randn(*s, dtype=torch.bfloat16) * 0.1)(D, N)
        B_re = (lambda *s: torch.randn(*s, dtype=torch.bfloat16) * 0.2)(D, N)
        B_im = (lambda *s: torch.randn(*s, dtype=torch.bfloat16) * 0.2)(D, N)
        C_re = (lambda *s: torch.randn(*s, dtype=torch.bfloat16) * 0.2)(D, N)
        C_im = (lambda *s: torch.randn(*s, dtype=torch.bfloat16) * 0.2)(D, N)
        log_dt = (lambda *s: torch.randn(*s, dtype=torch.bfloat16) * 0.1)(D) * 0.5 - 4.0
        Dskip = (lambda *s: torch.randn(*s, dtype=torch.bfloat16) * 0.1)(D)
        return {'u': _w((lambda *s: torch.randn(*s, dtype=torch.bfloat16) * 0.05)(B, L, D)), 'A_re': _w(A_re), 'A_im': _w(A_im), 'B_re': _w(B_re), 'B_im': _w(B_im), 'C_re': _w(C_re), 'C_im': _w(C_im), 'log_dt': _w(log_dt), 'Dskip': _w(Dskip)}
    _A_in = _A_setup()

    def _B_setup():
        B, S, D, F = (1, 2048, 512, 1792)
        return {'x': _w((lambda *s: torch.randn(*s, dtype=torch.bfloat16) * 0.05)(B, S, D)), 'w1': _w((lambda *s: torch.randn(*s, dtype=torch.bfloat16) * 0.05)(D, F)), 'b1': _w((lambda *s: torch.randn(*s, dtype=torch.bfloat16) * 1.6)(F)), 'w2': _w((lambda *s: torch.randn(*s, dtype=torch.bfloat16) * 0.05)(F, D)), 'b2': _w((lambda *s: torch.randn(*s, dtype=torch.bfloat16) * 12.8)(D))}
    _B_in = _B_setup()
    return {'A_u': _A_in['u'], 'A_A_re': _A_in['A_re'], 'A_A_im': _A_in['A_im'], 'A_B_re': _A_in['B_re'], 'A_B_im': _A_in['B_im'], 'A_C_re': _A_in['C_re'], 'A_C_im': _A_in['C_im'], 'A_log_dt': _A_in['log_dt'], 'A_Dskip': _A_in['Dskip'], 'B_x': _B_in['x'], 'B_w1': _B_in['w1'], 'B_b1': _B_in['b1'], 'B_w2': _B_in['w2'], 'B_b2': _B_in['b2'], 'c': _w((lambda *s: torch.randn(*s, dtype=torch.bfloat16) * 0.2)(1, 512) * 8.0), 'w_mod': _w((lambda *s: torch.randn(*s, dtype=torch.bfloat16) * 0.2)(512, 6 * 512) * 512 ** (-0.5) * 8.0)}

def reference(A_u, A_A_re, A_A_im, A_B_re, A_B_im, A_C_re, A_C_im, A_log_dt, A_Dskip, B_x, B_w1, B_b1, B_w2, B_b2, c, w_mod):

    def _A_ref(u, A_re, A_im, B_re, B_im, C_re, C_im, log_dt, Dskip):
        Bsz, L, D = u.shape
        dt = torch.exp(log_dt)[:, None]
        A = A_re + 1j * A_im
        Bc = B_re + 1j * B_im
        Cc = C_re + 1j * C_im
        logdA = dt * A
        dB = dt * Bc
        l = (lambda x, dt: x.to(dt))(torch.arange(L), A_re.dtype)
        P = torch.exp(logdA[:, :, None] * l[None, None, :])
        K = torch.einsum('dn,dnl,dn->dl', Cc, P, dB).real
        L2 = 2 * L
        Kp = _pad(K, ((0, 0), (0, L)))
        up = (lambda x, *d: x.permute(*d))(u, 0, 2, 1)
        up = _pad(up, ((0, 0), (0, 0), (0, L)))
        Kf = (lambda x, axes=(-2, -1): torch.fft.rfft2(x.float(), dim=axes))(Kp[:, None, :], axes=(-2, -1))
        Uf = (lambda x, axes=(-2, -1): torch.fft.rfft2(x.float(), dim=axes))(up[:, :, None, :], axes=(-2, -1))
        Yf = Uf * Kf[None, :, :, :]
        y = (lambda x, s, axes=(-2, -1): torch.fft.irfft2(x, s=s, dim=axes).to(torch.bfloat16))(Yf, (1, L2), axes=(-2, -1))[:, :, 0, :L]
        y = (lambda x, *d: x.permute(*d))(y, 0, 2, 1) + u * Dskip[None, None, :]
        return y

    def _B_ref(x, w1, b1, w2, b2):
        h = torch.einsum('bsd,df->bsf', x, w1) + b1
        zero = torch.zeros_like(h)
        r = torch.where(h > zero, h, zero)
        h = r * r
        out = torch.einsum('bsf,fd->bsd', h, w2) + b2
        return out
    x = A_u
    mod = (_silu(c) @ w_mod).reshape(c.shape[0], 6, 1, -1)
    sa, ca, ga, sb, cb, gb = (mod[:, 0].clone(), mod[:, 1].clone(), mod[:, 2].clone(), mod[:, 3].clone(), mod[:, 4].clone(), mod[:, 5].clone())
    h = _layernorm(x, axis=-1) * (1 + ca) + sa
    x = x + ga * _A_ref(h, A_A_re, A_A_im, A_B_re, A_B_im, A_C_re, A_C_im, A_log_dt, A_Dskip)
    h = _layernorm(x, axis=-1) * (1 + cb) + sb
    x = x + gb * _B_ref(h, B_w1, B_b1, B_w2, B_b2)
    return x
```

```cpp
#include <hip/hip_runtime.h>
#include <hip/hip_bf16.h>
#include <stdint.h>

typedef unsigned short u16;
static constexpr int L = 2048, D = 512, N = 128, F = 1792;

__device__ __forceinline__ float bf2f(u16 v) { return __uint_as_float(((unsigned)v) << 16); }
__device__ __forceinline__ u16 f2bf(float f) { __hip_bfloat16 b = __float2bfloat16(f); return *(u16*)&b; }
__device__ __forceinline__ float rbf(float f) { return bf2f(f2bf(f)); }

__global__ void k_mod(const u16* c, const u16* wmod, float* mod) {
    int j = blockIdx.x * blockDim.x + threadIdx.x;
    float acc = 0.f;
    for (int k = 0; k < D; ++k) {
        float cv = bf2f(c[k]);
        float s = rbf(cv / (1.f + __expf(-cv)));
        acc += s * bf2f(wmod[k * 3072 + j]);
    }
    mod[j] = rbf(acc);
}

__global__ void k_kgen(const u16* Are, const u16* Aim, const u16* Bre, const u16* Bim, const u16* Cre, const u16* Cim,
                       const u16* logdt, float* K) {
    int d = blockIdx.y;
    int l = blockIdx.x * blockDim.x + threadIdx.x;
    float lf = rbf((float)l);
    float dt = rbf(expf(bf2f(logdt[d])));
    float acc = 0.f;
    for (int n = 0; n < N; ++n) {
        int i = d * N + n;
        float ar = dt * bf2f(Are[i]), ai = dt * bf2f(Aim[i]);
        float br = dt * bf2f(Bre[i]), bi = dt * bf2f(Bim[i]);
        float cr = bf2f(Cre[i]), ci = bf2f(Cim[i]);
        float wr = cr * br - ci * bi, wi = cr * bi + ci * br;
        float e = expf(ar * lf);
        float s, co;
        sincosf(ai * lf, &s, &co);
        acc += e * (wr * co - wi * s);
    }
    K[d * L + l] = acc;
}

__global__ void k_ln(const u16* x, const float* mod, int sidx, int cidx, u16* h) {
    int l = blockIdx.x, t = threadIdx.x;
    float v[8];
    float s = 0.f;
    for (int i = 0; i < 8; ++i) { v[i] = bf2f(x[l * D + t * 8 + i]); s += v[i]; }
    for (int o = 32; o; o >>= 1) s += __shfl_xor(s, o);
    float mean = s / D;
    float q = 0.f;
    for (int i = 0; i < 8; ++i) { float dd = v[i] - mean; q += dd * dd; }
    for (int o = 32; o; o >>= 1) q += __shfl_xor(q, o);
    float var = rbf(q / D);
    float mb = rbf(mean);
    float den = rbf(sqrtf(rbf(var + 1e-5f)));
    for (int i = 0; i < 8; ++i) {
        int d = t * 8 + i;
        float xn = rbf(rbf(v[i] - mb) / den);
        float hv = rbf(rbf(xn * rbf(1.f + mod[cidx * D + d])) + mod[sidx * D + d]);
        h[l * D + d] = f2bf(hv);
    }
}

__global__ void k_conv(const u16* x, const u16* h, const float* K, const float* mod, const u16* Dskip, u16* x1) {
    int d = blockIdx.x * blockDim.x + threadIdx.x;
    int l = blockIdx.y;
    float acc = 0.f;
    for (int j = 0; j <= l; ++j) acc += K[d * L + l - j] * bf2f(h[j * D + d]);
    float y = rbf(acc);
    float hv = bf2f(h[l * D + d]);
    y = rbf(y + rbf(hv * bf2f(Dskip[d])));
    x1[l * D + d] = f2bf(bf2f(x[l * D + d]) + rbf(mod[2 * D + d] * y));
}

__global__ void k_g1(const u16* h2, const u16* w1, const u16* b1, u16* H) {
    int f = blockIdx.x * blockDim.x + threadIdx.x;
    int l = blockIdx.y;
    float acc = 0.f;
    for (int k = 0; k < D; ++k) acc += bf2f(h2[l * D + k]) * bf2f(w1[k * F + f]);
    float v = rbf(rbf(acc) + bf2f(b1[f]));
    v = v > 0.f ? v : 0.f;
    H[l * F + f] = f2bf(v * v);
}

__global__ void k_g2(const u16* H, const u16* w2, const u16* b2, const u16* x1, const float* mod, u16* out) {
    int d = blockIdx.x * blockDim.x + threadIdx.x;
    int l = blockIdx.y;
    float acc = 0.f;
    for (int k = 0; k < F; ++k) acc += bf2f(H[l * F + k]) * bf2f(w2[k * D + d]);
    float v = rbf(rbf(acc) + bf2f(b2[d]));
    out[l * D + d] = f2bf(bf2f(x1[l * D + d]) + rbf(mod[5 * D + d] * v));
}

extern "C" void kernel_launch(void* const* d_in, const int* in_sizes, int n_in,
                              void* d_out, int out_size, void* d_ws, size_t ws_size,
                              hipStream_t stream) {
    const u16* u = (const u16*)d_in[0];
    const u16* Are = (const u16*)d_in[1];
    const u16* Aim = (const u16*)d_in[2];
    const u16* Bre = (const u16*)d_in[3];
    const u16* Bim = (const u16*)d_in[4];
    const u16* Cre = (const u16*)d_in[5];
    const u16* Cim = (const u16*)d_in[6];
    const u16* logdt = (const u16*)d_in[7];
    const u16* Dskip = (const u16*)d_in[8];
    const u16* w1 = (const u16*)d_in[10];
    const u16* b1 = (const u16*)d_in[11];
    const u16* w2 = (const u16*)d_in[12];
    const u16* b2 = (const u16*)d_in[13];
    const u16* c = (const u16*)d_in[14];
    const u16* wmod = (const u16*)d_in[15];
    char* ws = (char*)d_ws;
    float* mod = (float*)ws;
    float* K = (float*)(ws + (1 << 20));
    u16* h = (u16*)(ws + (8 << 20));
    u16* x1 = (u16*)(ws + (12 << 20));
    u16* h2 = (u16*)(ws + (16 << 20));
    u16* H = (u16*)(ws + (20 << 20));
    k_mod<<<3072 / 64, 64, 0, stream>>>(c, wmod, mod);
    k_kgen<<<dim3(L / 256, D), 256, 0, stream>>>(Are, Aim, Bre, Bim, Cre, Cim, logdt, K);
    k_ln<<<L, 64, 0, stream>>>(u, mod, 0, 1, h);
    k_conv<<<dim3(D / 256, L), 256, 0, stream>>>(u, h, K, mod, Dskip, x1);
    k_ln<<<L, 64, 0, stream>>>(x1, mod, 3, 4, h2);
    k_g1<<<dim3(F / 256, L), 256, 0, stream>>>(h2, w1, b1, H);
    k_g2<<<dim3(D / 256, L), 256, 0, stream>>>(H, w2, b2, x1, mod, (u16*)d_out);
}
```

```cpp
#include <hip/hip_runtime.h>
#include <stdint.h>
#include <type_traits>

typedef unsigned short u16;
typedef unsigned v4u __attribute__((ext_vector_type(4)));
typedef unsigned v2u __attribute__((ext_vector_type(2)));
typedef float f2 __attribute__((ext_vector_type(2)));
typedef float f4 __attribute__((ext_vector_type(4)));
typedef float f16v __attribute__((ext_vector_type(16)));
typedef short s4 __attribute__((ext_vector_type(4)));
typedef __bf16 bf2 __attribute__((ext_vector_type(2)));
typedef __bf16 bf8 __attribute__((ext_vector_type(8)));
typedef _Float16 h2 __attribute__((ext_vector_type(2)));
typedef _Float16 h8 __attribute__((ext_vector_type(8)));
typedef __amdgpu_buffer_rsrc_t rsrc_t;
#define LAS __attribute__((address_space(3)))
#define DEV __device__ __forceinline__

#define CUTS 0

struct Args {
    const u16 *x, *Are, *Aim, *Bre, *Bim, *Cre, *Cim, *ldt, *Dsk, *w1, *b1, *w2, *b2, *c, *wm;
    u16* out; char* ws; unsigned long long* st;
};

constexpr unsigned O_E1 = 0, O_E2 = 256, O_ST = 4096, O_MD = 65536, O_F2 = 196608, O_F3 = 262144, O_PS = 524288;
constexpr unsigned O_XT = 2u << 20, O_Z = 4u << 20, O_X1 = 6u << 20, O_H = 8u << 20, WSB = 16u << 20;
constexpr unsigned L_XY = 28672, XY_W = 15504, XY_T = 4352, KI_C = 4160, KI_CH = 2 * XY_W, L_MS = L_XY + 4 * XY_W, L_SI = L_MS + 16384, L_T0 = L_SI + 512;
constexpr unsigned L_H = 114688, H_CH = 4608, L_EX = 123904, L_XS = L_EX + 16384, L_T1 = L_XS + 8192;
constexpr unsigned L_MV = 156672, L_CS = L_MV + 128, CS_B = 1152, L_GB = L_CS + 2304, L_AP = L_GB + 1024, L_ZR = L_AP + 896, L_BT = L_AP + 1024, L_R = L_BT + 1024, L_SP = L_R + 512;
constexpr unsigned L_FLG = 163840 - 128, LDS_BYTES = 163840;
constexpr unsigned W2_W = 34816;
constexpr int NA = 6, NR = 22;
enum { F_XTD = 0, F_KP0, F_KP1, F_KE0, F_KE1, F_AF, F_H, F_EX, F_ZD, F_E2, F_W1, F_ST, F_RD, F_BASE };
constexpr unsigned SPIN = 1u << 17;

DEV float asf(unsigned v) { return __uint_as_float(v); }
DEV unsigned asu(float v) { return __float_as_uint(v); }
DEV float blo(unsigned v) { return asf(v << 16); }
DEV float bhi(unsigned v) { return asf(v & 0xffff0000u); }
DEV unsigned pkb(float a, float b) { bf2 r = __builtin_convertvector((f2){a, b}, bf2); return __builtin_bit_cast(unsigned, r); }
DEV unsigned pkh(f2 a) { h2 r = __builtin_convertvector(a, h2); return __builtin_bit_cast(unsigned, r); }
DEV float rbf(float a) { return blo(pkb(a, a)); }
DEV unsigned rfl(unsigned v) { return __builtin_amdgcn_readfirstlane(v); }
template <int C> DEV float dppf(float v) { return asf((unsigned)__builtin_amdgcn_update_dpp(0, (int)asu(v), C, 0xf, 0xf, false)); }
DEV float red16(float v) { v += dppf<0x128>(v); v += dppf<0x124>(v); v += dppf<0x122>(v); v += dppf<0x121>(v); return v; }
DEV float sw32add(float a, float b) { v2u r = __builtin_amdgcn_permlane32_swap(asu(a), asu(b), false, false); unsigned x = r.x, y = r.y; return asf(x) + asf(y); }
DEV float sw16add(float a, float b) { v2u r = __builtin_amdgcn_permlane16_swap(asu(a), asu(b), false, false); unsigned x = r.x, y = r.y; return asf(x) + asf(y); }
DEV rsrc_t mk(const void* p, unsigned n) { return __builtin_amdgcn_make_buffer_rsrc((void*)p, 0, n, 0x00020000); }
DEV void dma(rsrc_t r, unsigned voff, unsigned soff, unsigned ldst) {
    asm volatile("s_mov_b32 m0, %0\n\ts_nop 0\n\tbuffer_load_dwordx4 %1, %2, %3 offen lds" :: "s"(ldst), "v"(voff), "s"(r), "s"(soff) : "memory");
}
#define EARLY 1
#define SETM0(v) asm volatile("s_mov_b32 m0, %0\n\ts_nop 0" :: "s"(v) : "memory")
#define WTID(v, off) asm volatile("ds_write_addtid_b32 %0 offset:%1" :: "v"(v), "i"(off) : "memory")
#define SB() __builtin_amdgcn_sched_barrier(0)
#define VM(n) asm volatile("s_waitcnt vmcnt(%0)" :: "i"(n) : "memory")
#define LG0() asm volatile("s_waitcnt lgkmcnt(0)" ::: "memory")
#define CFENCE() asm volatile("" ::: "memory")
#define LD4(r, vo, so) __builtin_amdgcn_raw_buffer_load_b128(r, vo, so, 16)
#define LD2(r, vo, so) __builtin_amdgcn_raw_buffer_load_b64(r, vo, so, 16)
#define LD1(r, vo, so) __builtin_amdgcn_raw_buffer_load_b32(r, vo, so, 16)
#define ST4(v, r, vo, so) __builtin_amdgcn_raw_buffer_store_b128(v, r, (vo) + (so), 0, 16)
#define ST1(v, r, vo, so) __builtin_amdgcn_raw_buffer_store_b32(v, r, vo, so, 16)
#define L4(a) (*(LAS v4u*)(lds + (a)))
#define L2(a) (*(LAS v2u*)(lds + (a)))
#define L1(a) (*(LAS unsigned*)(lds + (a)))
#define LF4(a) (*(LAS f4*)(lds + (a)))
#define LF2(a) (*(LAS f2*)(lds + (a)))
#define LF1(a) (*(LAS float*)(lds + (a)))
typedef LAS char* lptr;
DEV v2u trd(lptr lds, unsigned a) { s4 v = __builtin_amdgcn_ds_read_tr16_b64_v4i16((LAS s4*)(lds + a)); return __builtin_bit_cast(v2u, v); }
DEV v4u trd2(lptr lds, unsigned a, unsigned b) { v2u p = trd(lds, a), q = trd(lds, b); return (v4u){p.x, p.y, q.x, q.y}; }
DEV f4 mm16(v4u a, v4u b, f4 c) { return __builtin_amdgcn_mfma_f32_16x16x32_bf16(__builtin_bit_cast(bf8, a), __builtin_bit_cast(bf8, b), c, 0, 0, 0); }
DEV f4 mm16h(v4u a, v4u b, f4 c) { return __builtin_amdgcn_mfma_f32_16x16x32_f16(__builtin_bit_cast(h8, a), __builtin_bit_cast(h8, b), c, 0, 0, 0); }
DEV f4 mm16s(v2u a, v2u b, f4 c) { return __builtin_amdgcn_mfma_f32_16x16x16bf16_1k(__builtin_bit_cast(s4, a), __builtin_bit_cast(s4, b), c, 0, 0, 0); }
DEV f16v mm32(v4u a, v4u b, f16v c) { return __builtin_amdgcn_mfma_f32_32x32x16_bf16(__builtin_bit_cast(bf8, a), __builtin_bit_cast(bf8, b), c, 0, 0, 0); }

DEV void sig(lptr lds, int i) {
    const unsigned ad = (unsigned)(size_t)lds + L_FLG + i * 4, one = 1;
    asm volatile("s_waitcnt lgkmcnt(0)\n\ts_mov_b64 exec, 1\n\tds_add_u32 %0, %1\n\ts_mov_b64 exec, -1" :: "v"(ad), "v"(one) : "memory");
}
DEV unsigned sigr(lptr lds, int i) {
    const unsigned ad = (unsigned)(size_t)lds + L_FLG + i * 4, one = 1; unsigned o;
    asm volatile("s_waitcnt lgkmcnt(0)\n\ts_mov_b64 exec, 1\n\tds_add_rtn_u32 %0, %1, %2\n\ts_mov_b64 exec, -1\n\ts_waitcnt lgkmcnt(0)" : "=&v"(o) : "v"(ad), "v"(one) : "memory");
    return rfl(o);
}
template <int SLEEP> DEV void wts(lptr lds, int i, unsigned n) {
    for (unsigned s = 0; s < (SPIN << 6); ++s) {
        unsigned v = __hip_atomic_load((LAS unsigned*)(lds + L_FLG) + i, __ATOMIC_RELAXED, __HIP_MEMORY_SCOPE_WORKGROUP);
        if (rfl(v) >= n) break;
        if (SLEEP) __builtin_amdgcn_s_sleep(SLEEP);
    }
    CFENCE();
}
#define wt wts<0>
#define wtl wts<1>
DEV f2 cm(f2 a, f2 m, f2 mp) { f2 t = m * (f2){a.x, a.x}; return __builtin_elementwise_fma(mp, (f2){a.y, a.y}, t); }
DEV f2 perp(f2 m) { return (f2){-m.y, m.x}; }
DEV f2 cexpn(float lr2, float rev, float p) {
    float mg = __builtin_amdgcn_exp2f(lr2 * p), ph = __builtin_amdgcn_fractf(rev * p);
    return (f2){mg * __builtin_amdgcn_cosf(ph), mg * __builtin_amdgcn_sinf(ph)};
}

#define STAMP(i) do {} while (0)
#define STAMP_DECL
#define STAMP_OUT(base) do {} while (0)

#define W1P(i) dma(rw1, vo1[(i) % 7], ((((i) / 7 + rot) & 3) * 4 + j) * (32 * 3584), (((i) / 7 * 4 + j) * 7 + (i) % 7) * 1024)
#define VO1() _Pragma("unroll") for (int i = 0; i < 7; ++i) { int c = i * 64 + lane; vo1[i] = (c / 14) * 3584 + mem1 * 224 + (c % 14) * 16; }
template <int PB, int PE>
__global__ void __launch_bounds__(512, 2) fused(Args a) {
    constexpr bool hasA = PB == 0, hasB = PB <= 1 && PE >= 1, hasC = PB <= 2 && PE >= 2, hasD = PE == 3;
    constexpr bool fuse1 = hasA && hasB, fuse2 = hasB && hasC, fuse3 = hasC && hasD;
    extern __shared__ __attribute__((aligned(16))) char smem[];
    lptr lds = (lptr)smem;
    const int w = rfl(threadIdx.x >> 6), lane = threadIdx.x & 63, j = w & 3;
    const int xcd = blockIdx.x & 7, ix = blockIdx.x >> 3, b = xcd * 32 + ix;
    const int team1 = (xcd >> 1) * 4 + (ix >> 3), mem1 = (xcd & 1) * 8 + (ix & 7);
    const int team2 = b >> 4, th = (b >> 3) & 1, cb2 = b & 7, rot = ix >> 3;
    const int q = lane >> 4, r16 = lane & 15;
    const rsrc_t rw = mk(a.ws, WSB);
    STAMP_DECL
    STAMP(PB == 0 ? 0 : PB == 1 ? 13 : PB == 2 ? 14 : 15);

    if (w >= 4) {
        const unsigned ep = LD1(rw, 0, PB == 0 ? O_E1 : O_E2);
        const rsrc_t rw1 = mk(a.w1, 512 * 1792 * 2), rw2 = mk(a.w2, 512 * 1792 * 2);
        const unsigned w2vo = (lane >> 3) * 1024 + cb2 * 128 + (((lane & 7) ^ (((lane >> 4) & 1) << 2)) * 16);
        const unsigned w2ra = (q >> 1) * 1024 + (r16 >> 2) * 128 + (((2 * (q & 1) + ((r16 & 3) >> 1)) ^ ((r16 >> 3) << 2)) * 16) + 8 * (r16 & 1);
        unsigned vo1[7];
        v4u wf[NR];
#define W2P(i, dst) dma(rw2, w2vo, (j * 56 + (rot * 14 + (i)) % 56) * 8192, dst)
#define W2R(i, src) do { wf[i] = trd2(lds, (src) + w2ra, (src) + w2ra + 512); wf[(i) + 1] = trd2(lds, (src) + (w2ra ^ 64), (src) + (w2ra ^ 64) + 512); } while (0)
        v4u xa[4]; unsigned cv = 0;
        const int tile = ix * 2 + (j & 1), tb = xcd * 8 + (tile >> 3), cbx = tile & 7;
        const bool domod = ix < 24;
        const int kq = (ix & 1) * 2 + (j & 1);
        if constexpr (hasA) {
            const rsrc_t rx = mk(a.x, 2048 * 512 * 2);
            if (j < 2) {
#pragma unroll
                for (int i = 0; i < 4; ++i) dma(rx, (lane >> 3) * 1024 + (((lane & 7) ^ ((2 * i + (lane >> 5)) & 7)) * 16), (tb * 32 + 8 * i) * 1024 + cbx * 128, L_XS + j * 4096 + i * 1024);
            } else {
#pragma unroll
                for (int i = 0; i < 4; ++i) asm volatile("buffer_load_dwordx4 %0, %1, %2, %3 offen offset:%4" : "=v"(xa[i]) : "v"(q * 1024 + r16 * 16), "s"(rx), "s"((b * 8 + (j & 1) * 4) * 1024), "i"(i * 256) : "memory");
                asm volatile("global_load_dword %0, %1, off" : "=v"(cv) : "v"(a.c + kq * 128 + lane * 2) : "memory");
            }
        }
        if constexpr (hasC) { VO1(); }
        if (threadIdx.x < 256 + 32) L1(L_FLG + (threadIdx.x & 31) * 4) = 0;
        LG0();
        __builtin_amdgcn_s_barrier();
        if constexpr (hasD && !hasC) {
#pragma unroll
            for (int i = 0; i < NR; ++i) W2P(i, j * W2_W + i * 1024);
        }
        unsigned tag = hasA ? ep + 1 : ep;
        if constexpr (hasA) {
            asm volatile("s_waitcnt vmcnt(0)" : "+v"(tag), "+v"(cv), "+v"(xa[0]), "+v"(xa[1]), "+v"(xa[2]), "+v"(xa[3]) :: "memory");
            if (j < 2) {
                v4u t4[4];
#pragma unroll
                for (int i = 0; i < 4; ++i) t4[i] = L4(L_XS + j * 4096 + (4 * (lane & 7) + i) * 128 + (((lane >> 3) ^ (lane & 7)) * 16));
#pragma unroll
                for (int i = 0; i < 4; ++i) { v4u g = {t4[0][i], t4[1][i], t4[2][i], t4[3][i]};
                    ST4(g, rw, ((lane >> 3) * 4 + i) * 8192 + (lane & 7) * 16, O_XT + cbx * 32 * 8192 + tb * 128); }
                STAMP(1);
                VM(0);
                if (j == 0) sig(lds, F_XTD);
                else {
                    wt(lds, F_XTD, 3);
                    v2u p = L2(L_SP + (lane & 3) * 8);
                    if (lane < 4) { v4u g = {p.x, tag, p.y, tag}; ST4(g, rw, lane * 16, O_ST + b * 128); }
                }
                STAMP(2);
            } else {
                if (domod) {
                    const rsrc_t rm = mk(a.wm, 512 * 3072 * 2);
#pragma unroll
                    for (int i = 0; i < 8; ++i) dma(rm, (lane >> 2) * 6144 + (lane & 3) * 16, (kq * 128 + i * 16) * 6144 + (xcd * 12 + (ix >> 1)) * 64, L_MS + (j & 1) * 8192 + i * 1024);
                }
                f2 v[16]; f2 s2 = {0.f, 0.f};
#pragma unroll
                for (int i = 0; i < 4; ++i)
#pragma unroll
                    for (int e = 0; e < 4; ++e) { unsigned u = xa[i][e]; v[i * 4 + e] = (f2){blo(u), bhi(u)}; s2 += v[i * 4 + e]; }
                const float mean = red16(s2.x + s2.y) * (1.f / 512.f);
                f2 q2 = {0.f, 0.f};
#pragma unroll
                for (int i = 0; i < 16; ++i) { f2 dd = v[i] - mean; q2 = __builtin_elementwise_fma(dd, dd, q2); }
                const float var = rbf(red16(q2.x + q2.y) * (1.f / 512.f));
                const float den = __builtin_amdgcn_sqrtf(rbf(var + 1e-5f));
                if (r16 == 0) L1(L_SP + ((j & 1) * 4 + q) * 4) = pkb(mean, den);
                sig(lds, F_XTD);
                if (domod) {
                    float c0 = blo(cv), c1 = bhi(cv);
                    float s0 = c0 * __builtin_amdgcn_rcpf(1.f + __builtin_amdgcn_exp2f(-1.44269504f * c0));
                    float s1 = c1 * __builtin_amdgcn_rcpf(1.f + __builtin_amdgcn_exp2f(-1.44269504f * c1));
                    L1(L_SI + (j & 1) * 256 + lane * 4) = pkb(s0, s1);
                    v4u bs[4];
#pragma unroll
                    for (int s = 0; s < 4; ++s) bs[s] = L4(L_SI + (j & 1) * 256 + s * 64 + q * 16);
                    const unsigned ma = L_MS + (j & 1) * 8192 + (8 * q + (r16 >> 2)) * 64 + (r16 & 3) * 8;
                    VM(0);
                    v4u am[8];
#pragma unroll
                    for (int s = 0; s < 8; ++s) am[s] = trd2(lds, ma + (s >> 1) * 2048 + (s & 1) * 32, ma + (s >> 1) * 2048 + (s & 1) * 32 + 256);
                    SB();
                    f4 d0 = {0, 0, 0, 0}, d1 = {0, 0, 0, 0};
#pragma unroll
                    for (int s = 0; s < 4; ++s) { d0 = mm16(am[2 * s], bs[s], d0); d1 = mm16(am[2 * s + 1], bs[s], d1); }
                    f4 d = (r16 & 2) ? d1 : d0;
                    float e0 = (r16 & 1) ? d[2] : d[0], e1 = (r16 & 1) ? d[3] : d[1];
                    if (r16 < 4) { v4u g = {asu(e0), tag, asu(e1), tag};
                        ST4(g, rw, ((r16 >> 1) * 16 + q * 4 + (r16 & 1) * 2) * 8, O_MD + kq * 24576 + (xcd * 12 + (ix >> 1)) * 256); }
                }
            }
            if constexpr (PE < 3) { if (b == 0 && threadIdx.x == 256 + 128) { unsigned e2 = LD1(rw, 0, O_E2); ST1(e2 + 1, rw, 0, O_E2); } }
        }
        SB();
        STAMP(3);
        if constexpr (hasB) {
            v4u s[4], md;
            const unsigned dk = *(const unsigned*)(a.Dsk + 2 * b);
            const unsigned mo = (lane & 3) * 24576 + (min(lane >> 2, 5) * 512 + 2 * b) * 8;
            bool okm[4] = {false, false, false, false};
            for (unsigned n = 0; n < SPIN; ++n) {
                CFENCE();
#pragma unroll
                for (int i = 0; i < 4; ++i) if (!okm[i]) s[i] = LD4(rw, (lane >> 2) * 128 + (lane & 3) * 16 + i * 2048, O_ST + j * 8192);
                if constexpr (!fuse1) break;
#pragma unroll
                for (int i = 0; i < 4; ++i) okm[i] = (s[i].y == tag) & (s[i].w == tag);
                if (__all(okm[0] & okm[1] & okm[2] & okm[3])) break;
                __builtin_amdgcn_s_sleep(1);
            }
            CFENCE();
            STAMP(4);
            v2u xt[4];
#pragma unroll
            for (int i = 0; i < 4; ++i) xt[i] = LD2(rw, lane * 8 + i * 512, O_XT + b * 8192 + j * 2048);
            for (unsigned n = 0; n < SPIN; ++n) {
                CFENCE();
                md = LD4(rw, mo, O_MD);
                if constexpr (!fuse1) break;
                if (__all((md.y == tag) & (md.w == tag))) break;
                __builtin_amdgcn_s_sleep(1);
            }
            CFENCE();
            float m0 = asf(md.x), m1 = asf(md.z);
            m0 += dppf<0xB1>(m0); m1 += dppf<0xB1>(m1);
            m0 += dppf<0x4E>(m0); m1 += dppf<0x4E>(m1);
            const bool one = ((lane >> 2) == 1) | ((lane >> 2) == 4);
            unsigned mp = pkb(m0, m1);
            if (one) mp = pkb(1.f + blo(mp), 1.f + bhi(mp));
            const unsigned psa = __builtin_amdgcn_readlane(mp, 0), pca = __builtin_amdgcn_readlane(mp, 4), pga = __builtin_amdgcn_readlane(mp, 8);
            if (j == 0 && lane < 28 && (lane & 3) == 0) L1(L_MV + lane) = lane < 24 ? mp : tag;
            SETM0(61440 + j * 1024);
            const f2 sa = {blo(psa), bhi(psa)}, ca = {blo(pca), bhi(pca)}, gd = {blo(pga) * blo(dk), bhi(pga) * bhi(dk)};
#pragma unroll
            for (int i = 0; i < 4; ++i) {
                f2 x0 = {blo(xt[i].x), bhi(xt[i].x)}, x1 = {blo(xt[i].y), bhi(xt[i].y)};
                float r0 = __builtin_amdgcn_rcpf(bhi(s[i].x)), r1 = __builtin_amdgcn_rcpf(bhi(s[i].z));
                f2 h0 = __builtin_elementwise_fma((x0 - blo(s[i].x)) * r0, ca, sa), h1 = __builtin_elementwise_fma((x1 - blo(s[i].z)) * r1, ca, sa);
                unsigned pa = pkb(h0.x, h1.x), pb = pkb(h0.y, h1.y);
                const unsigned t2 = j * 512 + i * 128 + lane * 2;
                WTID(pa, L_H + 512 - 61440 + i * 256); WTID(pb, L_H + H_CH + 512 - 61440 + i * 256);
                LF2(L_EX + t2 * 4) = (f2){__builtin_fmaf(gd.x, blo(pa), x0.x), __builtin_fmaf(gd.x, bhi(pa), x1.x)};
                LF2(L_EX + 8192 + t2 * 4) = (f2){__builtin_fmaf(gd.y, blo(pb), x0.y), __builtin_fmaf(gd.y, bhi(pb), x1.y)};
            }
            sig(lds, F_H);
            STAMP(5);
            if constexpr (fuse2) {
                wtl(lds, F_AF, 4);
                if constexpr (hasA) wt(lds, F_H, 4);
                const unsigned stg = (12 + j) * 7168;
                if constexpr (!EARLY) {
#pragma unroll
                    for (int i = 0; i < 7; ++i) W1P(i);
                }
                if constexpr (fuse3) {
#pragma unroll
                    for (int i = 0; i < NA; ++i) W2P(i, stg + i * 1024);
                }
#pragma unroll
                for (int i = 7; i < 21; ++i) W1P(i);
                if constexpr (fuse3) {
                    VM(14);
#pragma unroll
                    for (int i = 0; i < NA; i += 2) W2R(i, stg + i * 1024);
                    LG0();
                }
#pragma unroll
                for (int i = 21; i < 28; ++i) W1P(i);
            }
            if constexpr (fuse1) { if (b == 0 && j == 0) { wt(lds, F_H, 4); if (lane == 0) ST1(tag, rw, 0, O_E1); } }
        }
        if constexpr (hasC || hasD) {
            if constexpr (fuse2) { VM(0); sig(lds, F_W1); }
            STAMP(6);
            if constexpr (fuse2) wtl(lds, F_ZD, 4);
            v4u f0; unsigned f1x, f1y;
            bool ok = false;
            for (unsigned n = 0; n < SPIN; ++n) {
                CFENCE();
                if (!ok) { f0 = LD4(rw, lane * 128, O_F2 + j * 8192); v2u f1 = LD2(rw, lane * 128 + 16, O_F2 + j * 8192); f1x = f1.x; f1y = f1.y; }
                if constexpr (!fuse2) break;
                ok = (f0.y == tag) & (f0.w == tag) & (f1y == tag);
                if (__all(ok)) break;
            }
            CFENCE();
            v4u sx1[2];
            const unsigned x1o = (lane >> 3) * 512 + (lane & 7) * 16, x1b = O_X1 + team1 * 131072 + mem1 * 8192 + j * 128;
            if constexpr (hasC && !fuse2) {
                sx1[0] = LD4(rw, x1o, x1b); sx1[1] = LD4(rw, x1o + 4096, x1b);
                asm volatile("" : "+v"(f0), "+v"(sx1[0]), "+v"(sx1[1]));
#pragma unroll
                for (int i = 0; i < 28; ++i) W1P(i);
            }
            const unsigned cso = (((j - rot) & 3) * 64 + lane) * 4;
            L1(L_CS + cso) = f0.x; L1(L_CS + CS_B + cso) = f0.z; L1(L_GB + (j * 64 + lane) * 4) = f1x;
            sig(lds, F_E2);
            wt(lds, F_E2, 4);
            STAMP(7);
          if constexpr (hasC) {
            if constexpr (fuse2) { sx1[0] = LD4(rw, x1o, x1b); sx1[1] = LD4(rw, x1o + 4096, x1b); }
            v2u b1v, b1w;
            auto stats_pub = [&]() {
                float sm[4], sq[4];
#pragma unroll
                for (int e = 0; e < 4; ++e) {
                    unsigned u0 = sx1[0][e], u1 = sx1[1][e];
                    float v0 = blo(u0), v1 = bhi(u0), v2 = blo(u1), v3 = bhi(u1);
                    sm[e] = (v0 + v1) + (v2 + v3);
                    sq[e] = __builtin_fmaf(v0, v0, v1 * v1) + __builtin_fmaf(v2, v2, v3 * v3);
                }
                float sA = sw32add(sm[0], sm[2]), sB = sw32add(sm[1], sm[3]), qA = sw32add(sq[0], sq[2]), qB = sw32add(sq[1], sq[3]);
                float s1 = sw16add(sA, sB), q1 = sw16add(qA, qB);
                s1 += dppf<0x128>(s1); q1 += dppf<0x128>(q1);
                if (!(lane & 8)) { v4u g = {asu(s1), tag, asu(q1), tag}; ST4(g, rw, (q * 8 + (lane & 7)) * 16, O_PS + team1 * 32768 + mem1 * 2048 + j * 512); }
                SB();
            };
            b1v = *(const v2u*)(a.b1 + mem1 * 112 + j * 16 + q * 4);
            b1w = *(const v2u*)(a.b1 + mem1 * 112 + (j < 3 ? j + 4 : j) * 16 + q * 4);
            if constexpr (!fuse2) { stats_pub(); VM(0); sig(lds, F_W1); }
            SB();
            wt(lds, F_W1, 4);
            f4 d0 = {0, 0, 0, 0}, d1 = {0, 0, 0, 0};
            {
                unsigned wa = (4 * q + (r16 >> 2)) * 224 + (r16 & 3) * 8 + j * 32, wb = wa + (j < 3 ? 128 : 0);
                const unsigned ca = L_CS + (r16 & 1) * CS_B + q * 8;
#pragma unroll
                for (int hf = 0; hf < 2; ++hf) {
#pragma unroll
                    for (int g = 0; g < 2; ++g) {
                        v4u bs[4], a0[4], a1[4];
#pragma unroll
                        for (int u = 0; u < 4; ++u) {
                            const int ks = hf * 8 + g * 4 + u, o = (g * 4 + u) * 7168;
                            v2u c0 = *(volatile LAS v2u*)(lds + ca + ks * 64), c1 = *(volatile LAS v2u*)(lds + ca + ks * 64 + 32);
                            bs[u] = (v4u){c0.x, c0.y, c1.x, c1.y};
                            a0[u] = trd2(lds, wa + o, wa + o + 3584); a1[u] = trd2(lds, wb + o, wb + o + 3584);
                        }
                        SB();
#pragma unroll
                        for (int u = 0; u < 4; ++u) { d0 = mm16(a0[u], bs[u], d0); d1 = mm16(a1[u], bs[u], d1); }
                    }
                    wa += 57344; wb += 57344; asm volatile("" : "+v"(wa), "+v"(wb));
                }
            }
            if constexpr (fuse2) stats_pub();
            constexpr int R0 = fuse2 ? NA : 0;
            if constexpr (fuse3) {
#pragma unroll
                for (int i = 0; i < 8; ++i) W2P(R0 + i, L_H + j * 8192 + i * 1024);
            }
            if (r16 == 1) {
                d0 += (f4){blo(b1v.x), bhi(b1v.x), blo(b1v.y), bhi(b1v.y)};
                d1 += (f4){blo(b1w.x), bhi(b1w.x), blo(b1w.y), bhi(b1w.y)};
            }
            if (r16 < 2) {
#pragma unroll
                for (int e = 0; e < 4; ++e) {
                    float h = rbf(d0[e]); L1(L_AP + (j * 16 + q * 4 + e) * 8 + r16 * 4) = pkb(h, d0[e] - h);
                    if (j < 3) { float g = rbf(d1[e]); L1(L_AP + ((j + 4) * 16 + q * 4 + e) * 8 + r16 * 4) = pkb(g, d1[e] - g); }
                }
            }
            if (lane < 3) L1(L_ZR + lane * 4) = lane < 2 ? 0 : tag;
            if constexpr (fuse3) {
                VM(0);
#pragma unroll
                for (int i = 0; i < 8; i += 2) W2R(R0 + i, L_H + j * 8192 + i * 1024);
            }
            STAMP(8);
            v4u p8[8];
            for (unsigned n = 0; n < SPIN; ++n) {
                CFENCE();
#pragma unroll
                for (int i = 0; i < 8; ++i) p8[i] = LD4(rw, (lane >> 5) * 2048 + (lane & 31) * 16, O_PS + team1 * 32768 + j * 512 + i * 4096);
                bool ok2 = true;
#pragma unroll
                for (int i = 0; i < 8; ++i) ok2 &= (p8[i].y == tag) & (p8[i].w == tag);
                if (__all(ok2)) break;
                __builtin_amdgcn_s_sleep(2);
            }
            CFENCE();
            if constexpr (fuse3) {
                LG0();
#pragma unroll
                for (int i = 8; i < 16; ++i) W2P(R0 + i, L_H + j * 8192 + (i - 8) * 1024);
            }
            float ss = 0.f, sx = 0.f;
#pragma unroll
            for (int i = 0; i < 8; ++i) { ss += asf(p8[i].x); sx += asf(p8[i].z); }
            ss = sw32add(ss, ss); sx = sw32add(sx, sx);
            const float mean = ss * (1.f / 512.f);
            const float var = rbf(__builtin_fmaxf(__builtin_fmaf(-mean, mean, sx * (1.f / 512.f)), 0.f));
            const float den = rbf(__builtin_amdgcn_sqrtf(rbf(var + 1e-5f)));
            const int e = (lane >> 3) & 3, u = 2 * (lane & 7) + (e >> 1) + 16 * (e & 1);
            if (lane < 32) { L2(L_BT + (j * 32 + u) * 8) = (v2u){pkb(-mean, -mean), pkb(den, den)}; LF1(L_R + (j * 32 + u) * 4) = __builtin_amdgcn_rcpf(den); }
            sig(lds, F_ST);
            STAMP(9);
            if constexpr (fuse3) {
                VM(0);
#pragma unroll
                for (int i = 8; i < 16; i += 2) W2R(R0 + i, L_H + j * 8192 + (i - 8) * 1024);
                if constexpr (R0 + 16 < NR) {
                    LG0();
#pragma unroll
                    for (int i = 16; R0 + i < NR; ++i) W2P(R0 + i, L_H + j * 8192 + (i - 16) * 1024);
                    VM(0);
#pragma unroll
                    for (int i = 16; R0 + i < NR; i += 2) W2R(R0 + i, L_H + j * 8192 + (i - 16) * 1024);
                }
                LG0();
            }
          }
        }
        if constexpr (hasD) {
            if constexpr (fuse3) __builtin_amdgcn_s_barrier();
            else {
                VM(0);
#pragma unroll
                for (int i = 0; i < NR; i += 2) W2R(i, j * W2_W + i * 1024);
                LG0();
            }
#pragma unroll
            for (int i = NR; i < 56; ++i) W2P(i, j * W2_W + (i - NR) * 1024);
            const int dt = j >> 1, tq = j & 1, u = lane & 31, hh = lane >> 5;
            unsigned xr[8];
            {
                const unsigned xo = (2 * hh) * 512 + ((u & 15) >> 1) * 16 + (2 * (u & 1) + (u >> 4)) * 4;
#pragma unroll
                for (int i = 0; i < 8; ++i) xr[i] = LD1(rw, xo + ((i >> 1) * 4 + (i & 1)) * 512, O_X1 + team2 * 131072 + (cb2 * 32 + dt * 16) * 512 + (2 * th + tq) * 128);
            }
            v2u b2v[4];
#pragma unroll
            for (int i = 0; i < 4; ++i) b2v[i] = *(const v2u*)(a.b2 + cb2 * 64 + dt * 32 + i * 8 + hh * 4);
            STAMP(10);
            if constexpr (fuse3) {
                for (unsigned n = 0; n < SPIN; ++n) {
                    CFENCE();
                    unsigned f = LD1(rw, ((lane & 3) * 4 + ((lane >> 2) & 1)) * 128, O_F3 + (team2 * 64 + j * 16 + th * 2) * 128);
                    if (__all(f == tag)) break;
                    __builtin_amdgcn_s_sleep(1);
                }
                CFENCE();
            }
            STAMP(11);
            f16v c2[4];
#pragma unroll
            for (int i = 0; i < 4; ++i)
#pragma unroll
                for (int e = 0; e < 16; ++e) c2[i][e] = 0.f;
            {
                const unsigned hb = O_H + team2 * 458752 + th * 229376 + j * 28672;
#define HWIN 5
                constexpr int HW = HWIN;
                v4u h[HW][2];
                v4u wl[2][2];
#define HL1(g, i) h[(g) % HW][i] = LD4(rw, lane * 16 + (i) * 114688, hb + ((rot * 7 + (g)) % 28) * 1024)
#pragma unroll
                for (int g = 0; g < HW - 1; ++g) { HL1(g, 0); SB(); HL1(g, 1); SB(); }
#pragma unroll
                for (int g = 0; g < 28; ++g) {
                    const int gn = g + 1;
                    v4u a0, a1;
                    if (2 * g < NR) { a0 = wf[2 * g]; a1 = wf[2 * g + 1]; } else { a0 = wl[g & 1][0]; a1 = wl[g & 1][1]; }
                    const unsigned src = j * W2_W + (2 * gn - NR) * 1024;
                    c2[0] = mm32(a0, h[g % HW][0], c2[0]);
                    if (g + HW - 1 < 28) HL1(g + HW - 1, 0);
                    SB();
                    c2[2] = mm32(a1, h[g % HW][0], c2[2]);
                    if (2 * gn >= NR && gn < 28) wl[gn & 1][0] = trd2(lds, src + w2ra, src + w2ra + 512);
                    SB();
                    c2[1] = mm32(a0, h[g % HW][1], c2[1]);
                    if (g + HW - 1 < 28) HL1(g + HW - 1, 1);
                    SB();
                    c2[3] = mm32(a1, h[g % HW][1], c2[3]);
                    if (2 * gn >= NR && gn < 28) wl[gn & 1][1] = trd2(lds, src + (w2ra ^ 64), src + (w2ra ^ 64) + 512);
                    SB();
                }
            }
            STAMP(12);
            f16v own;
#pragma unroll
            for (int i = 0; i < 4; ++i) {
                if (i == j) own = c2[i];
                else {
#pragma unroll
                    for (int e = 0; e < 4; ++e) LF4(j * W2_W + i * 4096 + e * 1024 + lane * 16) = (f4){c2[i][4 * e], c2[i][4 * e + 1], c2[i][4 * e + 2], c2[i][4 * e + 3]};
                }
            }
            sig(lds, F_RD);
            wt(lds, F_RD, 4);
            v2u o[4];
#pragma unroll
            for (int e = 0; e < 4; ++e) {
                f4 v = {own[4 * e], own[4 * e + 1], own[4 * e + 2], own[4 * e + 3]};
                f4 p1 = LF4(((j + 1) & 3) * W2_W + j * 4096 + e * 1024 + lane * 16), p2 = LF4(((j + 2) & 3) * W2_W + j * 4096 + e * 1024 + lane * 16), p3 = LF4(((j + 3) & 3) * W2_W + j * 4096 + e * 1024 + lane * 16);
                v = (v + p1) + (p2 + p3);
                v2u g = L2(L_GB + (cb2 * 32 + dt * 16 + e * 4 + hh * 2) * 4);
                v += (f4){blo(b2v[e].x), bhi(b2v[e].x), blo(b2v[e].y), bhi(b2v[e].y)};
                o[e].x = pkb(__builtin_fmaf(blo(g.x), v[0], blo(xr[2 * e])), __builtin_fmaf(bhi(g.x), v[1], bhi(xr[2 * e])));
                o[e].y = pkb(__builtin_fmaf(blo(g.y), v[2], blo(xr[2 * e + 1])), __builtin_fmaf(bhi(g.y), v[3], bhi(xr[2 * e + 1])));
            }
            const int tok = team2 * 128 + 16 * ((u & 15) >> 1) + 4 * (2 * th + tq) + 2 * (u & 1) + (u >> 4);
            u16* op = a.out + tok * 512 + cb2 * 64 + dt * 32 + hh * 8;
#pragma unroll
            for (int e = 0; e < 4; e += 2) {
                v2u rx_ = __builtin_amdgcn_permlane32_swap(o[e].x, o[e + 1].x, false, false);
                v2u ry_ = __builtin_amdgcn_permlane32_swap(o[e].y, o[e + 1].y, false, false);
                *(v4u*)(op + e * 8) = (v4u){rx_.x, ry_.x, rx_.y, ry_.y};
            }
        }
        STAMP_OUT(16);
        return;
    }

    const int ch = j >> 1, hf = j & 1;
    unsigned par[7];
    if constexpr (hasB) {
        const int pi = (2 * b + ch) * 128 + hf * 64 + lane;
        par[0] = a.Are[pi]; par[1] = a.Aim[pi]; par[2] = a.Bre[pi]; par[3] = a.Bim[pi]; par[4] = a.Cre[pi]; par[5] = a.Cim[pi]; par[6] = a.ldt[2 * b + ch];
        SB();
        const v4u z4 = {0, 0, 0, 0};
        if (threadIdx.x < 64) L4(L_H + (threadIdx.x >> 5) * H_CH + (threadIdx.x & 31) * 16) = z4;
        if (lane == 0) L2(ch ? L_T1 : L_T0) = (v2u){0, 0};
        LG0();
    }
    __builtin_amdgcn_s_barrier();
    if constexpr (hasB) {
        unsigned ta[19], wa_[12], vo1[7];
        if constexpr (fuse2 && EARLY) { VO1(); }
        {
            const int u = hf * 64 + lane;
            const unsigned tb_ = ch ? L_T1 : L_T0;
            const bool small = u >= 113;
#pragma unroll
            for (int e = 0; e < 20; e += 2) {
                const int l0 = 2047 - 16 * u - e;
                const unsigned p = pkb((float)l0, (float)(l0 - 1));
                int i0 = small ? l0 + 1 : (int)(p & 0xffff) - (0x4300 - 129), i1 = small ? l0 : (int)(p >> 16) - (0x4300 - 129);
                if (e >= 16) { i0 = max(i0, 0); i1 = max(i1, 0); }
                ta[e] = tb_ + (i0 + (i0 >> 4)) * 8; asm volatile("" : "+v"(ta[e]));
                if (e + 1 < 19) { ta[e + 1] = tb_ + (i1 + (i1 >> 4)) * 8; asm volatile("" : "+v"(ta[e + 1])); }
            }
#pragma unroll
            for (int t = 0; t < 3; ++t)
#pragma unroll
                for (int e = 0; e < 4; ++e) {
                    int v;
                    if (t == 0) v = 512 * q + 128 * e;
                    else if (t == 1) v = q < 2 ? 512 * q + 128 * e + 4 : 256 * (q - 2) + 128 * (e >> 1) + 2 + 4 * (e & 1);
                    else v = q < 2 ? 128 * q + 1 + 2 * e : 2048;
                    v += 8 * r16;
                    int idx = v < 256 ? v + 1 : (int)(asu((float)v) >> 16) - (0x4300 - 129);
                    if (t == 2 && q >= 2 && !(e == 0 && lane == 32)) idx = 642;
                    wa_[t * 4 + e] = tb_ + (idx + (idx >> 4)) * 8 + hf * 4; asm volatile("" : "+v"(wa_[t * 4 + e]));
                }
        }
        SB();
        const float dt = rbf(__builtin_amdgcn_exp2f(blo(par[6]) * 1.44269504f));
        STAMP(1);
        const float lr2 = dt * blo(par[0]) * 1.44269504f, rev = dt * blo(par[1]) * 0.15915494f;
        const float br = dt * blo(par[2]), bi = dt * blo(par[3]), cr = blo(par[4]), ci = blo(par[5]);
        f2 W = {cr * br - ci * bi, cr * bi + ci * br};
        unsigned mx = max(asu(W.x) & 0x7fffffffu, asu(W.y) & 0x7fffffffu);
        mx = max(mx, asu(dppf<0x128>(asf(mx)))); mx = max(mx, asu(dppf<0x124>(asf(mx)))); mx = max(mx, asu(dppf<0x122>(asf(mx)))); mx = max(mx, asu(dppf<0x121>(asf(mx))));
        const unsigned mu = max(max((unsigned)__builtin_amdgcn_readlane(mx, 0), (unsigned)__builtin_amdgcn_readlane(mx, 16)), max((unsigned)__builtin_amdgcn_readlane(mx, 32), (unsigned)__builtin_amdgcn_readlane(mx, 48)));
        const unsigned ex = min(max(mu >> 23, 30u), 230u);
        const float sc = asf((266u - ex) << 23), isc = asf((ex - 12u) << 23);
        SETM0(j * XY_W);
#define PUT(t, row, v) WTID(pkh(v), L_XY + (t) * XY_T + (row) * 272)
        {
            const f2 y8 = cexpn(lr2, -rev, 8.f), y8p = perp(y8);
            const f2 z1 = cexpn(lr2, rev, 1.f), z1p = perp(z1), z2 = cm(z1, z1, z1p), z2p = perp(z2), z4 = cm(z2, z2, z2p), z4p = perp(z4);
            const f2 zc = cexpn(lr2, rev, 128.f), zcp = perp(zc);
            f2 X = W * sc, y = {1.f, 0.f};
#pragma unroll
            for (int i = 0; i < 16; ++i) {
                const f2 Xn = cm(X, zc, zcp), yn = i == 0 ? y8 : cm(y, y8, y8p);
                PUT(1, i, X); PUT(0, i, y);
                if (i < 8) {
                    f2 X4 = cm(X, z4, z4p); PUT(2, i, X4);
                    if (i < 4) {
                        f2 X2 = cm(X, z2, z2p), X6 = cm(X4, z2, z2p); PUT(2, 8 + 2 * i, X2); PUT(2, 9 + 2 * i, X6);
                        if (i < 2) { PUT(3, 4 * i, cm(X, z1, z1p)); PUT(3, 4 * i + 1, cm(X2, z1, z1p)); PUT(3, 4 * i + 2, cm(X4, z1, z1p)); PUT(3, 4 * i + 3, cm(X6, z1, z1p)); }
                    }
                }
                X = Xn; y = yn;
            }
            PUT(3, 8, X);
        }
        SB();
        {
            const unsigned fa = L_XY + j * XY_W + r16 * 272 + q * 16;
            v4u fr[4][4];
#pragma unroll
            for (int s = 0; s < 4; ++s)
#pragma unroll
                for (int t = 0; t < 4; ++t) fr[s][t] = L4(fa + t * XY_T + s * 64);
            SB();
            f4 d[3] = {{0, 0, 0, 0}, {0, 0, 0, 0}, {0, 0, 0, 0}};
#pragma unroll
            for (int s = 0; s < 4; ++s)
#pragma unroll
                for (int t = 0; t < 3; ++t) d[t] = mm16h(fr[s][t + 1], fr[s][0], d[t]);
#pragma unroll
            for (int t = 0; t < 3; ++t)
#pragma unroll
                for (int e = 0; e < 4; ++e) LF1(wa_[t * 4 + e]) = d[t][e] * isc;
        }
        sig(lds, F_KP0 + ch);
        if constexpr (fuse2 && EARLY) {
            const rsrc_t rw1 = mk(a.w1, 512 * 1792 * 2);
#pragma unroll
            for (int i = 0; i < 7; ++i) W1P(i);
        }
        wt(lds, F_KP0 + ch, 2);
        STAMP(2);
        const unsigned kib = L_XY + ch * KI_CH;
        {
            f2 kp[19]; float kv[19];
#pragma unroll
            for (int e = 0; e < 19; ++e) kp[e] = LF2(ta[e]);
            SB();
#pragma unroll
            for (int e = 0; e < 19; ++e) kv[e] = kp[e].x + kp[e].y;
            const unsigned ka = kib + (hf * 64 + lane) * 32;
#pragma unroll
            for (int c = 0; c < 4; ++c)
#pragma unroll
                for (int hx = 0; hx < 2; ++hx) {
                    const int e = c + 8 * hx;
                    v4u pk_;
#pragma unroll
                    for (int i = 0; i < 4; ++i) { unsigned t_ = pkb(kv[e + 2 * i], kv[e + 2 * i + 1]); asm volatile("" : "+v"(t_)); pk_[i] = t_; }
                    L4(ka + c * KI_C + hx * 16) = pk_;
                }
            if (lane < 4) L4(kib + lane * KI_C + 4096 + hf * 16) = (v4u){0, 0, 0, 0};
        }
        sig(lds, F_KE0 + ch); wt(lds, F_KE0 + ch, 2);
        v4u af[4][8];
        {
            const unsigned aa = kib + ((3 - r16) & 3) * KI_C + 2 * (2028 - 4 * (r16 >> 2) + 8 * q - 128 * hf - 32 * 59);
#pragma unroll
            for (int d = 0; d < 4; ++d)
#pragma unroll
                for (int t = 0; t < 8; ++t) { const unsigned o = aa + 64 * (59 - d - 8 * t);
                    v2u p0 = *(volatile LAS v2u*)(lds + o), p1 = *(volatile LAS v2u*)(lds + o + 8); af[d][t] = (v4u){p0.x, p0.y, p1.x, p1.y}; }
        }
        sig(lds, F_AF);
        STAMP(3);
        wt(lds, F_H, 4);
        STAMP(4);
        __builtin_amdgcn_s_setprio(1);
        f4 bs[2][2];
        v4u mv0, mv1;
        f4 acc[8];
#pragma unroll
        for (int c = 0; c < 8; ++c) acc[c] = (f4){0, 0, 0, 0};
        {
            const unsigned ha = L_H + ch * H_CH + 2 * (240 + 16 * r16 + 8 * q - 128 * hf - 96);
            const unsigned ba = L_EX + (512 * j + 16 * r16 + 4 * q) * 4, ea = L_EX + j * 8192 + lane * 16;
            v4u bf[2][8];
#pragma unroll
            for (int e = 0; e < 8; ++e) { bf[0][e] = L4(ha + 64 * (8 * e + 3)); SB(); }
#pragma unroll
            for (int d = 0; d < 3; ++d) {
                int n = 0;
#pragma unroll
                for (int e = 0; e < 8; ++e)
#pragma unroll
                    for (int t = 0; t + e < 8; ++t) {
                        acc[t + e] = mm16(af[d][t], bf[d & 1][e], acc[t + e]);
                        if ((n & 3) == 1 && n < 32) bf[(d + 1) & 1][n >> 2] = L4(ha + 64 * (8 * (n >> 2) + 3 - (d + 1)));
                        if (d == 0 && (n & 3) == 3 && n < 16) bs[n >> 3][(n >> 2) & 1] = LF4(ba + (n >> 3) * 8192 + ((n >> 2) & 1) * 1024);
                        if (d == 0 && n == 19) mv0 = L4(L_MV);
                        if (d == 0 && n == 23) mv1 = L4(L_MV + 16);
                        ++n;
                        SB();
                    }
                if (d == 0) sig(lds, F_BASE);
                if (d == 2) wt(lds, F_BASE, 4);
            }
#pragma unroll
            for (int c = 7; c >= 0; --c) {
#pragma unroll
                for (int t = 0; t <= c; ++t) {
                    acc[c] = mm16(af[3][t], bf[1][c - t], acc[c]);
                    if (c < 7 && t == (c < 2 ? c : 2)) LF4(ea + (c + 1) * 1024) = acc[c + 1];
                    SB();
                }
            }
            LF4(ea) = acc[0];
        }
        __builtin_amdgcn_s_setprio(0);
        STAMP(5);
        sig(lds, F_EX); wt(lds, F_EX, 4);
        {
            const float ga0 = blo(mv0.z), ga1 = bhi(mv0.z), c10 = blo(mv1.x), c11 = bhi(mv1.x);
            const unsigned so = ((lane >> 3) & 1) * 131072 + q * 128 + (lane & 7) * 16;
            f4 p[2][4];
#pragma unroll
            for (int t = 0; t < 2; ++t)
#pragma unroll
                for (int i = 0; i < 4; ++i) p[t][i] = LF4(L_EX + i * 8192 + (2 * j + t) * 1024 + lane * 16);
            SB();
#pragma unroll
            for (int t = 0; t < 2; ++t) {
                f4 u0 = __builtin_elementwise_fma(p[t][0] + p[t][1], (f4){ga0, ga0, ga0, ga0}, bs[0][t]), u1 = __builtin_elementwise_fma(p[t][2] + p[t][3], (f4){ga1, ga1, ga1, ga1}, bs[1][t]);
                asm volatile("" : "+v"(u0), "+v"(u1));
                f4 s0 = u0 * c10, s1 = u1 * c11;
                asm volatile("" : "+v"(s0), "+v"(s1));
                v4u xo, zo;
#pragma unroll
                for (int e = 0; e < 4; ++e) { xo[e] = pkb(u0[e], u1[e]); zo[e] = pkb(s0[e], s1[e]); }
                ST4(zo, rw, so, O_Z + (4 * j + 2 * t) * 131072 + b * 512);
                ST4(xo, rw, so, O_X1 + (4 * j + 2 * t) * 131072 + b * 512);
            }
        }
        STAMP(6);
        if constexpr (fuse2) VM(0);
        if (sigr(lds, F_ZD) == 3) {
            if (lane < 2) { v4u g = {lane ? mv1.y : mv1.x, mv1.z, mv0.w, mv1.z}; ST4(g, rw, lane * 16, O_F2 + b * 128); }
        }
        STAMP(7);
    }
    if constexpr (hasC) {
        if constexpr (fuse2) wt(lds, F_E2, 4);
        STAMP(8);
        f4 c1[14];
#pragma unroll
        for (int i = 0; i < 14; ++i) c1[i] = (f4){0, 0, 0, 0};
        {
            const unsigned zo = q * 1024 + r16 * 8, zo2 = zo + 4096, zb = O_Z + team1 * 131072 + j * 128;
            unsigned wa = (4 * q + (r16 >> 2)) * 224 + (r16 & 3) * 8;
            v2u z[4][4];
            v2u wv[2][14];
#define ZL1(sl, i, so) z[sl][i] = LD2(rw, (((i) >> 1) ? zo2 : zo) + ((i) & 1) * 512, so)
            unsigned g0 = zb + (rot & 3) * 32768;
#pragma unroll
            for (int u = 0; u < 3; ++u)
#pragma unroll
                for (int i = 0; i < 4; ++i) { ZL1(u, i, g0 + u * 8192); SB(); }
            wt(lds, F_W1, 4);
            __builtin_amdgcn_s_setprio(1);
#pragma unroll
            for (int t = 0; t < 14; ++t) wv[0][t] = trd(lds, wa + (t >> 1) * 32 + (t & 1) * 3584);
            v4u fa = {z[0][0].x, z[0][1].x, z[0][2].x, z[0][3].x}, fb = {z[0][0].y, z[0][1].y, z[0][2].y, z[0][3].y};
            STAMP(9);
            auto body = [&](auto last_, unsigned gc, unsigned gn) {
                constexpr bool last = decltype(last_)::value;
#pragma unroll
                for (int u = 0; u < 4; ++u) {
                    v4u na, nb;
                    const int s1 = (u + 1) & 3;
#pragma unroll
                    for (int t = 0; t < 14; ++t) {
                        const v2u w0 = wv[u & 1][t & ~1], w1_ = wv[u & 1][t | 1];
                        c1[t] = mm16((v4u){w0.x, w0.y, w1_.x, w1_.y}, (t & 1) ? fb : fa, c1[t]);
                        if (!(last && u == 3)) wv[(u + 1) & 1][t] = trd(lds, wa + (u + 1) * 7168 + (t >> 1) * 32 + (t & 1) * 3584);
                        if (t % 3 == 1 && t < 12 && !(last && u > 0)) ZL1((u + 3) & 3, t / 3, u == 0 ? gc + 3 * 8192 : gn + (u - 1) * 8192);
                        if (t >= 9 && t < 13 && !(last && u == 3)) { na[t - 9] = z[s1][t - 9].x; nb[t - 9] = z[s1][t - 9].y; unsigned x_ = na[t - 9], y_ = nb[t - 9]; asm volatile("" : "+v"(x_), "+v"(y_)); na[t - 9] = x_; nb[t - 9] = y_; }
                        SB();
                    }
                    fa = na; fb = nb;
                }
            };
#pragma unroll 1
            for (int it = 0; it < 3; ++it) {
                const unsigned g1 = zb + ((it + 1 + rot) & 3) * 32768;
                body(std::false_type{}, g0, g1);
                g0 = g1; wa += 28672;
            }
            body(std::true_type{}, g0, g0);
            __builtin_amdgcn_s_setprio(0);
        }
        STAMP(10);
        wt(lds, F_ST, 4);
        if constexpr (fuse3) __builtin_amdgcn_s_barrier();
        {
            const v2u bA = L2(q ? L_ZR : L_BT + (j * 32 + r16) * 8), bB = L2(q ? L_ZR : L_BT + (j * 32 + 16 + r16) * 8);
            const float rA = LF1(L_R + (j * 32 + r16) * 4), rB = LF1(L_R + (j * 32 + 16 + r16) * 4);
            const unsigned hb = O_H + team1 * 458752 + j * 114688 + mem1 * 7168;
#pragma unroll
            for (int t = 0; t < 7; ++t) {
                const v2u ap = L2(L_AP + (t * 16 + r16) * 8);
                f4 uA = mm16s(ap, bA, c1[2 * t]) * rA, uB = mm16s(ap, bB, c1[2 * t + 1]) * rB;
#pragma unroll
                for (int e = 0; e < 4; ++e) { uA[e] = __builtin_fmaxf(uA[e], 0.f); uB[e] = __builtin_fmaxf(uB[e], 0.f); }
                uA *= uA; uB *= uB;
                v2u s0 = __builtin_amdgcn_permlane16_swap(pkb(uA[0], uA[1]), pkb(uB[0], uB[1]), false, false);
                v2u s1 = __builtin_amdgcn_permlane16_swap(pkb(uA[2], uA[3]), pkb(uB[2], uB[3]), false, false);
                ST4(((v4u){s0.x, s1.x, s0.y, s1.y}), rw, lane * 16, hb + t * 1024);
            }
        }
        STAMP(11);
        if constexpr (fuse3) {
            const unsigned tag = L1(L_ZR + 8);
            VM(0);
            if (lane == 0) ST1(tag, rw, 0, O_F3 + ((team1 * 16 + mem1) * 4 + j) * 128);
        }
        STAMP(12);
    }
    STAMP_OUT(0);
}

template <int PB, int PE>
static void launch(const Args& a, hipStream_t s) {
    (void)hipFuncSetAttribute((const void*)fused<PB, PE>, hipFuncAttributeMaxDynamicSharedMemorySize, LDS_BYTES);
    fused<PB, PE><<<256, 512, LDS_BYTES, s>>>(a);
}

extern "C" void kernel_launch(void* const* d_in, const int* in_sizes, int n_in, void* d_out, int out_size, void* d_ws, size_t ws_size, hipStream_t stream) {
    Args a;
    a.x = (const u16*)d_in[0]; a.Are = (const u16*)d_in[1]; a.Aim = (const u16*)d_in[2]; a.Bre = (const u16*)d_in[3]; a.Bim = (const u16*)d_in[4];
    a.Cre = (const u16*)d_in[5]; a.Cim = (const u16*)d_in[6]; a.ldt = (const u16*)d_in[7]; a.Dsk = (const u16*)d_in[8];
    a.w1 = (const u16*)d_in[10]; a.b1 = (const u16*)d_in[11]; a.w2 = (const u16*)d_in[12]; a.b2 = (const u16*)d_in[13];
    a.c = (const u16*)d_in[14]; a.wm = (const u16*)d_in[15];
    a.out = (u16*)d_out; a.ws = (char*)d_ws; a.st = (unsigned long long*)((char*)d_ws + (32u << 20));
    launch<0, 3>(a, stream);
}
```

```cpp
#include <hip/hip_runtime.h>
#include <stdint.h>
#include <type_traits>

typedef unsigned short u16;
typedef unsigned v4u __attribute__((ext_vector_type(4)));
typedef unsigned v2u __attribute__((ext_vector_type(2)));
typedef float f2 __attribute__((ext_vector_type(2)));
typedef float f4 __attribute__((ext_vector_type(4)));
typedef float f16v __attribute__((ext_vector_type(16)));
typedef short s4 __attribute__((ext_vector_type(4)));
typedef __bf16 bf2 __attribute__((ext_vector_type(2)));
typedef __bf16 bf8 __attribute__((ext_vector_type(8)));
typedef _Float16 h2 __attribute__((ext_vector_type(2)));
typedef _Float16 h8 __attribute__((ext_vector_type(8)));
typedef __amdgpu_buffer_rsrc_t rsrc_t;
#define LAS __attribute__((address_space(3)))
#define DEV __device__ __forceinline__

#define EARLY 0
#define CPF 0
#define ZWARM 1
#define PDLY 0
#define P2A 0
#define P2B 3
#define MEARLY 24
#define GP 2
#define E1MODE 1
#define PSD 10
#define WSEND 0
#define HWIN 5

struct Args {
    const u16 *x, *Are, *Aim, *Bre, *Bim, *Cre, *Cim, *ldt, *Dsk, *w1, *b1, *w2, *b2, *c, *wm;
    u16* out; char* ws; unsigned long long* st;
};

constexpr unsigned O_E1 = 0, O_ST = 4096, O_MD = 65536, O_F2 = 196608, O_F3 = 262144, O_PS = 524288;
constexpr unsigned O_Z = 4u << 20, O_X1 = 6u << 20, O_H = 8u << 20, O_XG = 16u << 20, WSB = 20u << 20;
constexpr unsigned L_XY = 28672, XY_W = 15504, XY_T = 4352, KI_C = 4160, KI_CH = 2 * XY_W, L_MS = L_XY + 4 * XY_W, L_SI = L_MS + 16384, L_T0 = L_SI + 512;
constexpr unsigned L_H = 114688, H_CH = 4608, L_EX = 123904, L_XS = L_EX + 16384, L_T1 = L_XS + 8192;
constexpr unsigned L_MV = 156672, L_CS = L_MV + 128, CS_B = 1152, L_GB = L_CS + 2304, L_AP = L_GB + 128, L_ZR = L_AP + 896, L_BT = L_AP + 1024, L_R = L_BT + 1024, L_SP = L_R + 512, L_JK = L_SP + 64, L_PJ = L_JK;
constexpr unsigned L_FLG = 163840 - 128, LDS_BYTES = 163840;
static_assert(L_JK + 1024 <= L_FLG, "lds");
constexpr unsigned W2_W = 38912;
constexpr int NA = 8, NR = 18;
enum { F_XTD = 0, F_KP0, F_KP1, F_KE0, F_KE1, F_AF, F_H, F_EX, F_ZD, F_E2, F_W1, F_ST, F_RD, F_BASE, F_MJ, F_PJ };
constexpr unsigned SPIN = 1u << 17;

DEV float asf(unsigned v) { return __uint_as_float(v); }
DEV unsigned asu(float v) { return __float_as_uint(v); }
DEV float blo(unsigned v) { return asf(v << 16); }
DEV float bhi(unsigned v) { return asf(v & 0xffff0000u); }
DEV unsigned pkb(float a, float b) { bf2 r = __builtin_convertvector((f2){a, b}, bf2); return __builtin_bit_cast(unsigned, r); }
DEV unsigned pkh(f2 a) { h2 r = __builtin_convertvector(a, h2); return __builtin_bit_cast(unsigned, r); }
DEV float rbf(float a) { return blo(pkb(a, a)); }
DEV unsigned rfl(unsigned v) { return __builtin_amdgcn_readfirstlane(v); }
template <int C> DEV float dppf(float v) { return asf((unsigned)__builtin_amdgcn_update_dpp(0, (int)asu(v), C, 0xf, 0xf, true)); }
DEV float red16(float v) { v += dppf<0x128>(v); v += dppf<0x124>(v); v += dppf<0x122>(v); v += dppf<0x121>(v); return v; }
DEV float sw32add(float a, float b) { v2u r = __builtin_amdgcn_permlane32_swap(asu(a), asu(b), false, false); unsigned x = r.x, y = r.y; return asf(x) + asf(y); }
DEV float sw16add(float a, float b) { v2u r = __builtin_amdgcn_permlane16_swap(asu(a), asu(b), false, false); unsigned x = r.x, y = r.y; return asf(x) + asf(y); }
DEV rsrc_t mk(const void* p, unsigned n) { return __builtin_amdgcn_make_buffer_rsrc((void*)p, 0, n, 0x00020000); }
DEV void dma(rsrc_t r, unsigned voff, unsigned soff, unsigned ldst) {
    asm volatile("s_mov_b32 m0, %0\n\ts_nop 0\n\tbuffer_load_dwordx4 %1, %2, %3 offen lds" :: "s"(ldst), "v"(voff), "s"(r), "s"(soff) : "memory");
}
DEV void dma1(rsrc_t r, unsigned voff, unsigned soff, unsigned ldst) {
    asm volatile("s_mov_b32 m0, %0\n\ts_nop 0\n\tbuffer_load_dwordx4 %1, %2, %3 offen sc1 lds" :: "s"(ldst), "v"(voff), "s"(r), "s"(soff) : "memory");
}
#define SETM0(v) asm volatile("s_mov_b32 m0, %0\n\ts_nop 0" :: "s"(v) : "memory")
#define WTID(v, off) asm volatile("ds_write_addtid_b32 %0 offset:%1" :: "v"(v), "i"(off) : "memory")
#define SB() __builtin_amdgcn_sched_barrier(0)
#define VM(n) asm volatile("s_waitcnt vmcnt(%0)" :: "i"(n) : "memory")
#define LG0() asm volatile("s_waitcnt lgkmcnt(0)" ::: "memory")
#define CFENCE() asm volatile("" ::: "memory")
#define LD4(r, vo, so) __builtin_amdgcn_raw_buffer_load_b128(r, vo, so, 16)
#define LD2(r, vo, so) __builtin_amdgcn_raw_buffer_load_b64(r, vo, so, 16)
#define LD1(r, vo, so) __builtin_amdgcn_raw_buffer_load_b32(r, vo, so, 16)
#define ST4(v, r, vo, so) __builtin_amdgcn_raw_buffer_store_b128(v, r, (vo) + (so), 0, 16)
#define ST1(v, r, vo, so) __builtin_amdgcn_raw_buffer_store_b32(v, r, vo, so, 16)
#define L4(a) (*(LAS v4u*)(lds + (a)))
#define L2(a) (*(LAS v2u*)(lds + (a)))
#define L2V(a) (*(volatile LAS v2u*)(lds + (a)))
#define L1(a) (*(LAS unsigned*)(lds + (a)))
#define LF4(a) (*(LAS f4*)(lds + (a)))
#define LF2(a) (*(LAS f2*)(lds + (a)))
#define LF1(a) (*(LAS float*)(lds + (a)))
typedef LAS char* lptr;
DEV v2u trd(lptr lds, unsigned a) { s4 v = __builtin_amdgcn_ds_read_tr16_b64_v4i16((LAS s4*)(lds + a)); return __builtin_bit_cast(v2u, v); }
DEV v4u trd2(lptr lds, unsigned a, unsigned b) { v2u p = trd(lds, a), q = trd(lds, b); return (v4u){p.x, p.y, q.x, q.y}; }
DEV f4 mm16(v4u a, v4u b, f4 c) { return __builtin_amdgcn_mfma_f32_16x16x32_bf16(__builtin_bit_cast(bf8, a), __builtin_bit_cast(bf8, b), c, 0, 0, 0); }
DEV f4 mm16h(v4u a, v4u b, f4 c) { return __builtin_amdgcn_mfma_f32_16x16x32_f16(__builtin_bit_cast(h8, a), __builtin_bit_cast(h8, b), c, 0, 0, 0); }
DEV f4 mm16s(v2u a, v2u b, f4 c) { return __builtin_amdgcn_mfma_f32_16x16x16bf16_1k(__builtin_bit_cast(s4, a), __builtin_bit_cast(s4, b), c, 0, 0, 0); }
DEV f16v mm32(v4u a, v4u b, f16v c) { return __builtin_amdgcn_mfma_f32_32x32x16_bf16(__builtin_bit_cast(bf8, a), __builtin_bit_cast(bf8, b), c, 0, 0, 0); }

DEV void sig(lptr lds, int i) {
    const unsigned ad = (unsigned)(size_t)lds + L_FLG + i * 4, one = 1;
    asm volatile("s_waitcnt lgkmcnt(0)\n\ts_mov_b64 exec, 1\n\tds_add_u32 %0, %1\n\ts_mov_b64 exec, -1" :: "v"(ad), "v"(one) : "memory");
}
DEV unsigned sigr(lptr lds, int i) {
    const unsigned ad = (unsigned)(size_t)lds + L_FLG + i * 4, one = 1; unsigned o;
    asm volatile("s_waitcnt lgkmcnt(0)\n\ts_mov_b64 exec, 1\n\tds_add_rtn_u32 %0, %1, %2\n\ts_mov_b64 exec, -1\n\ts_waitcnt lgkmcnt(0)" : "=&v"(o) : "v"(ad), "v"(one) : "memory");
    return rfl(o);
}
template <int SLEEP> DEV void wts(lptr lds, int i, unsigned n) {
    for (unsigned s = 0; s < (SPIN << 6); ++s) {
        unsigned v = __hip_atomic_load((LAS unsigned*)(lds + L_FLG) + i, __ATOMIC_RELAXED, __HIP_MEMORY_SCOPE_WORKGROUP);
        if (rfl(v) >= n) break;
        if (SLEEP) __builtin_amdgcn_s_sleep(SLEEP);
    }
    CFENCE();
}
#define wt wts<0>
#define wtl wts<1>
DEV f2 cm(f2 a, f2 m, f2 mp) { f2 t = m * (f2){a.x, a.x}; return __builtin_elementwise_fma(mp, (f2){a.y, a.y}, t); }
DEV f2 perp(f2 m) { return (f2){-m.y, m.x}; }
DEV f2 cexpn(float lr2, float rev, float p) {
    float mg = __builtin_amdgcn_exp2f(lr2 * p), ph = __builtin_amdgcn_fractf(rev * p);
    return (f2){mg * __builtin_amdgcn_cosf(ph), mg * __builtin_amdgcn_sinf(ph)};
}

#define STAMP(i) do {} while (0)
#define STAMP_DECL
#define STAMP_OUT(base) do {} while (0)

#define W1P(i) dma(rw1, vo1[(i) % 7], ((((i) / 7 + rot) & 3) * 4 + j) * (32 * 3584), (((i) / 7 * 4 + j) * 7 + (i) % 7) * 1024)
#define VO1() _Pragma("unroll") for (int i = 0; i < 7; ++i) { int c = i * 64 + lane; vo1[i] = (c / 14) * (3584 - 224) + c * 16 + mem1 * 224; }

__global__ void __launch_bounds__(512, 2) fused(Args a) {
    extern __shared__ __attribute__((aligned(16))) char smem[];
    lptr lds = (lptr)smem;
    const int w = rfl(threadIdx.x >> 6), lane = threadIdx.x & 63, j = w & 3;
    const int xcd = blockIdx.x & 7, ix = blockIdx.x >> 3, b = xcd * 32 + ix;
    const int team1 = (xcd >> 1) * 4 + (ix >> 3), mem1 = (xcd & 1) * 8 + (ix & 7);
    const int team2 = team1, th = (ix >> 2) & 1, cb2 = (xcd & 1) * 4 + (ix & 3), rot = ix >> 3, rk = ((ix >> 2) * 7) >> 1;
    const int q = lane >> 4, r16 = lane & 15;
    const rsrc_t rw = mk(a.ws, WSB);
    STAMP_DECL
    STAMP(0);

    if (w >= 4) {
        unsigned tag, ep;
        __builtin_amdgcn_s_setprio(2);
        asm volatile("buffer_load_dword %0, off, %1, 0 sc1" : "=v"(ep) : "s"(rw) : "memory");
        const rsrc_t rw1 = mk(a.w1, 512 * 1792 * 2), rw2 = mk(a.w2, 512 * 1792 * 2);
        unsigned vo1[7];
        v4u wf[NR];
#define W2P(i, dst) dma(rw2, w2vo, (j * 56 + (rk * 2 + (i)) % 56) * 8192, dst)
#define W2R(i, src) do { wf[i] = trd2(lds, (src) + w2ra, (src) + w2ra + 512); wf[(i) + 1] = trd2(lds, (src) + (w2ra ^ 64), (src) + (w2ra ^ 64) + 512); } while (0)
        v4u xa[4]; unsigned cv = 0;
        const bool domod = ix < 24;
        const int kq = (ix & 1) * 2 + (j & 1), mcb = ((ix >> 2) * 8 + xcd) * 2 + ((ix >> 1) & 1);
        {
            const rsrc_t rx = mk(a.x, 2048 * 512 * 2);
            if (j < 2) {
#pragma unroll
                for (int i = 0; i < 4; ++i) dma(rx, q * 1024 + ((r16 ^ (2 * i + (q >> 1))) * 16), ((b >> 1) * 16 + 4 * i) * 1024 + (b & 1) * 512 + j * 256, L_XS + j * 4096 + i * 1024);
            } else {
                asm volatile("global_load_dword %0, %1, off" : "=v"(cv) : "v"(a.c + kq * 128 + lane * 2) : "memory");
#pragma unroll
                for (int i = 0; i < 4; ++i) asm volatile("buffer_load_dwordx4 %0, %1, %2, %3 offen offset:%4" : "=v"(xa[i]) : "v"(q * 1024 + r16 * 16), "s"(rx), "s"((b * 8 + (j & 1) * 4) * 1024), "i"(i * 256) : "memory");
                const rsrc_t rm = mk(a.wm, ix < MEARLY ? 512 * 3072 * 2 : 0);
#pragma unroll
                for (int i = 0; i < 8; ++i) dma(rm, (lane >> 2) * 6144 + (lane & 3) * 16, (kq * 128 + i * 16) * 6144 + mcb * 64, L_MS + (j & 1) * 8192 + i * 1024);
            }
        }
        __builtin_amdgcn_s_barrier();
        if (j < 2) {
            asm volatile("s_waitcnt vmcnt(0)\n\tv_add_u32 %0, 1, %1" : "=v"(tag) : "v"(ep) : "memory");
            v4u t4[4];
#pragma unroll
            for (int i = 0; i < 4; ++i) t4[i] = L4(L_XS + j * 4096 + (2 * (lane & 7) + (i & 1)) * 256 + ((((lane >> 3) + 8 * (i >> 1)) ^ (lane & 7)) * 16));
#pragma unroll
            for (int i = 0; i < 8; ++i) { v4u g = {t4[(i >> 2) * 2][i & 3], tag, t4[(i >> 2) * 2 + 1][i & 3], tag};
                ST4(g, rw, ((lane >> 3) * 4 + (i & 3) + (i >> 2) * 32) * 16384 + (lane & 7) * 16, O_XG + ((b & 1) * 128 + j * 64) * 16384 + (b >> 1) * 128); }
            STAMP(1);
        } else {
            asm volatile("s_waitcnt vmcnt(8)\n\tv_add_u32 %0, 1, %6" : "=v"(tag), "+v"(cv), "+v"(xa[0]), "+v"(xa[1]), "+v"(xa[2]), "+v"(xa[3]) : "v"(ep) : "memory");
            STAMP(1);
            if (ix >= MEARLY && domod) {
                const rsrc_t rm = mk(a.wm, 512 * 3072 * 2);
#pragma unroll
                for (int i = 0; i < 8; ++i) dma(rm, (lane >> 2) * 6144 + (lane & 3) * 16, (kq * 128 + i * 16) * 6144 + mcb * 64, L_MS + (j & 1) * 8192 + i * 1024);
            }
            f2 v[16]; f2 s2 = {0.f, 0.f};
#pragma unroll
            for (int i = 0; i < 4; ++i)
#pragma unroll
                for (int e = 0; e < 4; ++e) { unsigned u = xa[i][e]; v[i * 4 + e] = (f2){blo(u), bhi(u)}; s2 += v[i * 4 + e]; }
            const float mean = red16(s2.x + s2.y) * (1.f / 512.f);
            f2 q2 = {0.f, 0.f};
#pragma unroll
            for (int i = 0; i < 16; ++i) { f2 dd = v[i] - mean; q2 = __builtin_elementwise_fma(dd, dd, q2); }
            const float var = rbf(red16(q2.x + q2.y) * (1.f / 512.f));
            const float den = __builtin_amdgcn_sqrtf(rbf(var + 1e-5f));
            if (r16 == 0) L1(L_SP + ((j & 1) * 4 + q) * 4) = pkb(mean, den);
            const bool pubd = sigr(lds, F_XTD) == 1;
            if (pubd) {
                v2u p = L2(L_SP + (lane & 3) * 8);
                if (lane < 4) { v4u g = {p.x, tag, p.y, tag}; ST4(g, rw, lane * 16, O_ST + b * 128); }
            }
            STAMP(14);
            float c0 = blo(cv), c1 = bhi(cv);
            float s0 = c0 * __builtin_amdgcn_rcpf(1.f + __builtin_amdgcn_exp2f(-1.44269504f * c0));
            float s1 = c1 * __builtin_amdgcn_rcpf(1.f + __builtin_amdgcn_exp2f(-1.44269504f * c1));
            L1(L_SI + (j & 1) * 256 + lane * 4) = pkb(s0, s1);
            v4u bs[4];
#pragma unroll
            for (int s = 0; s < 4; ++s) bs[s] = L4(L_SI + (j & 1) * 256 + s * 64 + q * 16);
            const unsigned ma = L_MS + (j & 1) * 8192 + (8 * q + (r16 >> 2)) * 64 + (r16 & 3) * 8;
            if (pubd) VM(1); else VM(0);
            SB();
            STAMP(15);
            v4u am[8];
#pragma unroll
            for (int s = 0; s < 8; ++s) { am[s] = trd2(lds, ma + (s >> 1) * 2048 + (s & 1) * 32, ma + (s >> 1) * 2048 + (s & 1) * 32 + 256); }
            SB();
            f4 d0 = {0, 0, 0, 0}, d1 = {0, 0, 0, 0};
#pragma unroll
            for (int s = 0; s < 4; ++s) { d0 = mm16(am[2 * s], bs[s], d0); d1 = mm16(am[2 * s + 1], bs[s], d1); }
            f4 d = (r16 & 2) ? d1 : d0;
            f2 e = {(r16 & 1) ? d[2] : d[0], (r16 & 1) ? d[3] : d[1]};
            LF2(L_MS + (j & 1) * 8192 + lane * 8) = e;
            if (sigr(lds, F_MJ) == 1) {
                e += LF2(L_MS + ((j & 1) ^ 1) * 8192 + lane * 8);
                if (domod && r16 < 4) { v4u g = {asu(e.x), tag, asu(e.y), tag};
                    ST4(g, rw, ((r16 >> 1) * 16 + q * 4 + (r16 & 1) * 2) * 8, O_MD + (ix & 1) * 24576 + mcb * 256); }
            }
            STAMP(2);
        }
        SB();
        const unsigned w2vo = (lane >> 3) * 1024 + cb2 * 128 + (((lane & 7) ^ (((lane >> 4) & 1) << 2)) * 16);
        const unsigned w2ra = (q >> 1) * 1024 + (r16 >> 2) * 128 + (((2 * (q & 1) + ((r16 & 3) >> 1)) ^ ((r16 >> 3) << 2)) * 16) + 8 * (r16 & 1);
        VO1();
        {
            v4u s[4], xg[4], md;
            const unsigned dk = *(const unsigned*)(a.Dsk + 2 * b);
            const unsigned mo = (lane & 1) * 24576 + (min(lane >> 2, 4) * 512 + 2 * b) * 8;
            if (j < 2) wtl(lds, F_XTD, 2);
            if (PDLY && j < 2) __builtin_amdgcn_s_sleep(PDLY);
            bool okm[4] = {false, false, false, false}, okd = false;
            if (E1MODE == 1) {
                for (unsigned n = 0; n < SPIN; ++n) {
                    CFENCE();
#pragma unroll
                    for (int i = 0; i < 4; ++i) if (!okm[i]) s[i] = LD4(rw, (lane >> 2) * 128 + (lane & 3) * 16 + i * 2048, O_ST + j * 8192);
#pragma unroll
                    for (int i = 0; i < 4; ++i) okm[i] = (s[i].y == tag) & (s[i].w == tag);
                    if (__all(okm[0] & okm[1] & okm[2] & okm[3])) break;
                    __builtin_amdgcn_s_sleep(3);
                }
#pragma unroll
                for (int i = 0; i < 4; ++i) okm[i] = false;
            }
            for (unsigned n = 0; n < SPIN; ++n) {
                CFENCE();
#pragma unroll
                for (int i = 0; i < 4; ++i) if (!okm[i]) { if (E1MODE == 0) s[i] = LD4(rw, (lane >> 2) * 128 + (lane & 3) * 16 + i * 2048, O_ST + j * 8192); xg[i] = LD4(rw, lane * 16 + i * 1024, O_XG + b * 16384 + j * 4096); }
                if (!okd) md = LD4(rw, mo, O_MD);
#pragma unroll
                for (int i = 0; i < 4; ++i) okm[i] = (s[i].y == tag) & (s[i].w == tag) & (xg[i].y == tag) & (xg[i].w == tag);
                okd = ((md.y == tag) & (md.w == tag)) | (lane >= 8);
                if (__all(okm[0] & okm[1] & okm[2] & okm[3] & okd)) break;
                __builtin_amdgcn_s_sleep(3);
            }
            CFENCE();
            STAMP(4);
            v2u xt[4];
#pragma unroll
            for (int i = 0; i < 4; ++i) xt[i] = (v2u){xg[i].x, xg[i].z};
            const bool one = ((lane >> 2) == 1) | ((lane >> 2) == 4);
            auto modv = [&]() { float m0 = asf(md.x), m1 = asf(md.z); m0 += dppf<0xB1>(m0); m1 += dppf<0xB1>(m1);
                unsigned mp = pkb(m0, m1); if (one) mp = pkb(1.f + blo(mp), 1.f + bhi(mp)); return mp; };
            unsigned mp = modv();
            const unsigned psa = __builtin_amdgcn_readlane(mp, 0), pca = __builtin_amdgcn_readlane(mp, 4);
            SETM0(61440 + j * 1024);
            const f2 sa = {blo(psa), bhi(psa)}, ca = {blo(pca), bhi(pca)};
#pragma unroll
            for (int i = 0; i < 4; ++i) {
                f2 x0 = {blo(xt[i].x), bhi(xt[i].x)}, x1 = {blo(xt[i].y), bhi(xt[i].y)};
                float r0 = __builtin_amdgcn_rcpf(bhi(s[i].x)), r1 = __builtin_amdgcn_rcpf(bhi(s[i].z));
                f2 h0 = __builtin_elementwise_fma((x0 - blo(s[i].x)) * r0, ca, sa), h1 = __builtin_elementwise_fma((x1 - blo(s[i].z)) * r1, ca, sa);
                unsigned pa = pkb(h0.x, h1.x), pb = pkb(h0.y, h1.y);
                WTID(pa, L_H + 512 - 61440 + i * 256); WTID(pb, L_H + H_CH + 512 - 61440 + i * 256);
                xt[i].x = pa; xt[i].y = pb; s[i] = (v4u){asu(x0.x), asu(x1.x), asu(x0.y), asu(x1.y)};
            }
            sig(lds, F_H);
            __builtin_amdgcn_s_setprio(0);
            STAMP(5);
            for (unsigned n = 0; n < SPIN; ++n) {
                if (__all((md.y == tag) & (md.w == tag))) break;
                CFENCE();
                md = LD4(rw, mo, O_MD);
            }
            mp = modv();
            const unsigned pga = __builtin_amdgcn_readlane(mp, 8), pcb = __builtin_amdgcn_readlane(mp, 16);
            if (j == 0 && lane == 0) L4(L_MV) = (v4u){pga, pcb, tag, 0};
            const f2 gd = {blo(pga) * blo(dk), bhi(pga) * bhi(dk)};
#pragma unroll
            for (int i = 0; i < 4; ++i) {
                const unsigned t2 = j * 512 + i * 128 + lane * 2;
                LF2(L_EX + t2 * 4) = (f2){__builtin_fmaf(gd.x, blo(xt[i].x), asf(s[i].x)), __builtin_fmaf(gd.x, bhi(xt[i].x), asf(s[i].y))};
                LF2(L_EX + 8192 + t2 * 4) = (f2){__builtin_fmaf(gd.y, blo(xt[i].y), asf(s[i].z)), __builtin_fmaf(gd.y, bhi(xt[i].y), asf(s[i].w))};
            }
            sig(lds, F_AF);
        }
        {
            v4u mc[5];
            const unsigned go = (lane & 1) * 24576 + (2560 + cb2 * 64 + (lane >> 1) * 2) * 8;
#pragma unroll
            for (int i = 0; i < 4; ++i) mc[i] = LD4(rw, lane * 16, O_MD + (i & 1) * 24576 + (3 + (i >> 1)) * 4096 + j * 1024);
            mc[4] = LD4(rw, go, O_MD);
            SB();
            if constexpr (!EARLY) {
#pragma unroll
                for (int i = 0; i < 7; ++i) W1P(i);
            }
            wtl(lds, F_AF, 8);
#pragma unroll
            for (int i = 7; i < 28; ++i) W1P(i);
            if (b == 0 && j == 0 && lane == 0) ST1(tag, rw, 0, O_E1);
            for (unsigned n = 0; n < SPIN; ++n) {
                bool ok = true;
#pragma unroll
                for (int i = 0; i < 5; ++i) ok &= (mc[i].y == tag) & (mc[i].w == tag);
                if (__all(ok)) break;
                CFENCE();
#pragma unroll
                for (int i = 0; i < 4; ++i) mc[i] = LD4(rw, lane * 16, O_MD + (i & 1) * 24576 + (3 + (i >> 1)) * 4096 + j * 1024);
                mc[4] = LD4(rw, go, O_MD);
            }
            const unsigned psb = pkb(asf(mc[0].x) + asf(mc[1].x), asf(mc[0].z) + asf(mc[1].z));
            const unsigned pcb = pkb(asf(mc[2].x) + asf(mc[3].x), asf(mc[2].z) + asf(mc[3].z));
            float g0 = asf(mc[4].x), g1 = asf(mc[4].z);
            g0 += dppf<0xB1>(g0); g1 += dppf<0xB1>(g1);
            const unsigned cso = (((j - rot) & 3) * 64 + lane) * 4;
            L1(L_CS + cso) = pkb(1.f + blo(pcb), 1.f + bhi(pcb)); L1(L_CS + CS_B + cso) = psb;
            L1(L_GB + (lane >> 1) * 4) = pkb(g0, g1);
            VM(0);
            sig(lds, F_W1);
            STAMP(6);
        }
        wtl(lds, F_E2, 4);
        __builtin_amdgcn_s_setprio(GP);
        STAMP(7);
        {
            v4u sx1[2];
#pragma unroll
            for (int i = 0; i < 2; ++i) sx1[i] = LD4(rw, (lane >> 3) * 512 + (lane & 7) * 16, O_X1 + team1 * 131072 + ((mem1 >> 2) * 64 + j * 16 + i * 8) * 512 + (mem1 & 3) * 128);
            float sm[4], sq[4];
#pragma unroll
            for (int e = 0; e < 4; ++e) {
                unsigned u0 = sx1[0][e], u1 = sx1[1][e];
                float v0 = blo(u0), v1 = bhi(u0), v2 = blo(u1), v3 = bhi(u1);
                sm[e] = (v0 + v1) + (v2 + v3);
                sq[e] = __builtin_fmaf(v0, v0, v1 * v1) + __builtin_fmaf(v2, v2, v3 * v3);
            }
            float sA = sw32add(sm[0], sm[2]), sB = sw32add(sm[1], sm[3]), qA = sw32add(sq[0], sq[2]), qB = sw32add(sq[1], sq[3]);
            float s1 = sw16add(sA, sB), q1 = sw16add(qA, qB);
            s1 += dppf<0x128>(s1); q1 += dppf<0x128>(q1);
            if (!(lane & 8)) LF2(L_PJ + j * 256 + (q * 8 + (lane & 7)) * 8) = (f2){s1, q1};
            if (sigr(lds, F_PJ) == 3) {
                const unsigned pa = L_PJ + (lane & 31) * 8;
                const f2 t = (LF2(pa) + LF2(pa + 256)) + (LF2(pa + 512) + LF2(pa + 768));
                if (lane < 32) { v4u g = {asu(t.x), tag, asu(t.y), tag}; ST4(g, rw, lane * 16, O_PS + team1 * 8192 + (mem1 & 3) * 2048 + (mem1 >> 2) * 512); }
            }
            SB();
            STAMP(8);
            __builtin_amdgcn_s_sleep(PSD);
            v4u p8[2];
            for (unsigned n = 0; n < SPIN; ++n) {
                CFENCE();
#pragma unroll
                for (int i = 0; i < 2; ++i) p8[i] = LD4(rw, lane * 16 + i * 1024, O_PS + team1 * 8192 + j * 2048);
                if (__all((p8[0].y == tag) & (p8[0].w == tag) & (p8[1].y == tag) & (p8[1].w == tag))) break;
                __builtin_amdgcn_s_sleep(4);
            }
            CFENCE();
            float ss = asf(p8[0].x) + asf(p8[1].x), sx = asf(p8[0].z) + asf(p8[1].z);
            ss = sw32add(ss, ss); sx = sw32add(sx, sx);
            const float mean = ss * (1.f / 512.f);
            const float var = rbf(__builtin_fmaxf(__builtin_fmaf(-mean, mean, sx * (1.f / 512.f)), 0.f));
            const float den = rbf(__builtin_amdgcn_sqrtf(rbf(var + 1e-5f)));
            const int e = (lane >> 3) & 3, u = (lane & 7) + 8 * (e >> 1) + 16 * (e & 1);
            if (lane < 32) { L2(L_BT + (j * 32 + u) * 8) = (v2u){pkb(-mean, -mean), pkb(den, den)}; LF1(L_R + (j * 32 + u) * 4) = __builtin_amdgcn_rcpf(den); }
            sig(lds, F_ST);
            __builtin_amdgcn_s_setprio(0);
            STAMP(9);
#pragma unroll
            for (int i = 0; i < 10; ++i) W2P(i, L_H + j * 10240 + i * 1024);
            VM(0);
#pragma unroll
            for (int i = 0; i < 10; i += 2) W2R(i, L_H + j * 10240 + i * 1024);
            LG0();
#pragma unroll
            for (int i = 10; i < NR; ++i) W2P(i, L_H + j * 10240 + (i - 10) * 1024);
            VM(0);
#pragma unroll
            for (int i = 10; i < NR; i += 2) W2R(i, L_H + j * 10240 + (i - 10) * 1024);
            sig(lds, F_RD);
            STAMP(3);
        }
        {
            wtl(lds, F_RD, 8);
#pragma unroll
            for (int i = NR; i < 56; ++i) W2P(i, j * W2_W + (i - NR) * 1024);
            const int dt = j >> 1, tq = j & 1, u = lane & 31, hh = lane >> 5;
            unsigned xr[8];
            {
                const unsigned xo = (2 * hh) * 512 + (u & 7) * 16 + (2 * ((u >> 3) & 1) + (u >> 4)) * 4;
#pragma unroll
                for (int i = 0; i < 8; ++i) xr[i] = LD1(rw, xo + ((i >> 1) & 1) * 2048 + (i & 1) * 512, O_X1 + team2 * 131072 + (cb2 * 32 + dt * 16) * 512 + (2 * th + tq) * 128 + (i >> 2) * 4096);
            }
            v2u b2v[4];
#pragma unroll
            for (int i = 0; i < 4; ++i) b2v[i] = *(const v2u*)(a.b2 + cb2 * 64 + dt * 32 + i * 8 + hh * 4);
            STAMP(10);
            for (unsigned n = 0; n < SPIN; ++n) {
                CFENCE();
                unsigned f = LD1(rw, ((lane & 3) * 4 + ((lane >> 2) & 1)) * 128, O_F3 + (team2 * 64 + j * 16 + th * 2) * 128);
                if (__all(f == tag)) break;
            }
            CFENCE();
            STAMP(11);
            f16v c2[4];
#pragma unroll
            for (int i = 0; i < 4; ++i)
#pragma unroll
                for (int e = 0; e < 16; ++e) c2[i][e] = 0.f;
            {
                const unsigned hb = O_H + team2 * 458752 + th * 229376 + j * 28672;
                constexpr int HW = HWIN;
                v4u h[HW][2];
                v4u wl[2][2];
                const unsigned hv0 = lane * 16, hv1 = lane * 16 + 114688, ra0 = j * W2_W + w2ra, ra1 = j * W2_W + (w2ra ^ 64);
#define HL1(g, i) h[(g) % HW][i] = LD4(rw, (i) ? hv1 : hv0, hb + ((rk + (g)) % 28) * 1024)
#pragma unroll
                for (int g = 0; g < HW - 1; ++g) { HL1(g, 0); SB(); HL1(g, 1); SB(); }
#pragma unroll
                for (int g = 0; g < 28; ++g) {
                    const int gn = g + 1;
                    v4u a0, a1;
                    if (2 * g < NR) { a0 = wf[2 * g]; a1 = wf[2 * g + 1]; } else { a0 = wl[g & 1][0]; a1 = wl[g & 1][1]; }
                    const unsigned src = (2 * gn - NR) * 1024;
                    c2[0] = mm32(a0, h[g % HW][0], c2[0]);
                    if (g + HW - 1 < 28) HL1(g + HW - 1, 0);
                    SB();
                    c2[2] = mm32(a1, h[g % HW][0], c2[2]);
                    if (2 * gn >= NR && gn < 28) wl[gn & 1][0] = trd2(lds, src + ra0, src + ra0 + 512);
                    SB();
                    c2[1] = mm32(a0, h[g % HW][1], c2[1]);
                    if (g + HW - 1 < 28) HL1(g + HW - 1, 1);
                    SB();
                    c2[3] = mm32(a1, h[g % HW][1], c2[3]);
                    if (2 * gn >= NR && gn < 28) wl[gn & 1][1] = trd2(lds, src + ra1, src + ra1 + 512);
                    SB();
                }
            }
            STAMP(12);
            f16v own;
#pragma unroll
            for (int i = 0; i < 4; ++i) {
                if (i == j) own = c2[i];
                else {
#pragma unroll
                    for (int e = 0; e < 4; ++e) LF4(j * W2_W + i * 4096 + e * 1024 + lane * 16) = (f4){c2[i][4 * e], c2[i][4 * e + 1], c2[i][4 * e + 2], c2[i][4 * e + 3]};
                }
            }
            sig(lds, F_RD);
            wt(lds, F_RD, 12);
            v2u o[4];
#pragma unroll
            for (int e = 0; e < 4; ++e) {
                f4 v = {own[4 * e], own[4 * e + 1], own[4 * e + 2], own[4 * e + 3]};
                f4 p1 = LF4(((j + 1) & 3) * W2_W + j * 4096 + e * 1024 + lane * 16), p2 = LF4(((j + 2) & 3) * W2_W + j * 4096 + e * 1024 + lane * 16), p3 = LF4(((j + 3) & 3) * W2_W + j * 4096 + e * 1024 + lane * 16);
                v = (v + p1) + (p2 + p3);
                v2u g = L2(L_GB + (dt * 16 + e * 4 + hh * 2) * 4);
                v += (f4){blo(b2v[e].x), bhi(b2v[e].x), blo(b2v[e].y), bhi(b2v[e].y)};
                o[e].x = pkb(__builtin_fmaf(blo(g.x), v[0], blo(xr[2 * e])), __builtin_fmaf(bhi(g.x), v[1], bhi(xr[2 * e])));
                o[e].y = pkb(__builtin_fmaf(blo(g.y), v[2], blo(xr[2 * e + 1])), __builtin_fmaf(bhi(g.y), v[3], bhi(xr[2 * e + 1])));
            }
            const int tok = team2 * 128 + 16 * (u & 7) + 4 * (2 * th + tq) + 2 * ((u >> 3) & 1) + (u >> 4);
            u16* op = a.out + tok * 512 + cb2 * 64 + dt * 32 + hh * 8;
#pragma unroll
            for (int e = 0; e < 4; e += 2) {
                v2u rx_ = __builtin_amdgcn_permlane32_swap(o[e].x, o[e + 1].x, false, false);
                v2u ry_ = __builtin_amdgcn_permlane32_swap(o[e].y, o[e + 1].y, false, false);
                *(v4u*)(op + e * 8) = (v4u){rx_.x, ry_.x, rx_.y, ry_.y};
            }
        }
        STAMP(13);
        STAMP_OUT(16);
        return;
    }

    const int ch = j >> 1, hf = j & 1;
    unsigned par[7];
    {
        const unsigned po = ((2 * b + ch) * 128 + hf * 64 + lane) * 2;
        asm volatile("global_load_ushort %0, %7, %8\n\tglobal_load_ushort %1, %7, %9\n\tglobal_load_ushort %2, %7, %10\n\tglobal_load_ushort %3, %7, %11\n\t"
                     "global_load_ushort %4, %7, %12\n\tglobal_load_ushort %5, %7, %13\n\tglobal_load_ushort %6, %14, %15"
                     : "=&v"(par[0]), "=&v"(par[1]), "=&v"(par[2]), "=&v"(par[3]), "=&v"(par[4]), "=&v"(par[5]), "=&v"(par[6])
                     : "v"(po), "s"(a.Are), "s"(a.Aim), "s"(a.Bre), "s"(a.Bim), "s"(a.Cre), "s"(a.Cim), "v"((2 * b + ch) * 2), "s"(a.ldt) : "memory");
        const v4u z4 = {0, 0, 0, 0};
        if (threadIdx.x < 64) L4(L_H + (threadIdx.x >> 5) * H_CH + (threadIdx.x & 31) * 16) = z4;
        if (lane == 0) L2(ch ? L_T1 : L_T0) = (v2u){0, 0};
        if (threadIdx.x >= 224) L1(L_FLG + (threadIdx.x & 31) * 4) = 0;
        LG0();
    }
    __builtin_amdgcn_s_barrier();
    unsigned tag;
    v2u b1v, b1w;
    {
        unsigned ta[19], wa_[12], vo1[7];
        const rsrc_t rw1 = mk(a.w1, 512 * 1792 * 2);
        if constexpr (EARLY) { VO1(); }
        if constexpr (EARLY == 2) {
#pragma unroll
            for (int i = 0; i < 7; ++i) W1P(i);
        }
        {
            const unsigned tb_ = (ch ? L_T1 : L_T0) + hf * 4;
#pragma unroll
            for (int t = 0; t < 3; ++t)
#pragma unroll
                for (int e = 0; e < 4; ++e) {
                    int v;
                    if (t == 0) v = 512 * q + 128 * e;
                    else if (t == 1) v = q < 2 ? 512 * q + 128 * e + 4 : 256 * (q - 2) + 128 * (e >> 1) + 2 + 4 * (e & 1);
                    else v = q < 2 ? 128 * q + 1 + 2 * e : 2048;
                    v += 8 * r16;
                    int idx = v < 256 ? v + 1 : (int)(asu((float)v) >> 16) - (0x4300 - 129);
                    if (t == 2 && q >= 2 && !(e == 0 && lane == 32)) idx = 642;
                    wa_[t * 4 + e] = tb_ + (idx + (idx >> 4)) * 8; asm volatile("" : "+v"(wa_[t * 4 + e]));
                }
        }
        {
            const int u = hf * 64 + lane;
            const unsigned tb_ = ch ? L_T1 : L_T0;
            const bool small = u >= 113;
#pragma unroll
            for (int e = 0; e < 20; e += 2) {
                const int l0 = 2047 - 16 * u - e;
                const unsigned p = pkb((float)l0, (float)(l0 - 1));
                int i0 = small ? l0 + 1 : (int)(p & 0xffff) - (0x4300 - 129), i1 = small ? l0 : (int)(p >> 16) - (0x4300 - 129);
                if (e >= 16) { i0 = max(i0, 0); i1 = max(i1, 0); }
                ta[e] = tb_ + (i0 + (i0 >> 4)) * 8; asm volatile("" : "+v"(ta[e]));
                if (e + 1 < 19) { ta[e + 1] = tb_ + (i1 + (i1 >> 4)) * 8; asm volatile("" : "+v"(ta[e + 1])); }
            }
        }
        asm volatile("s_waitcnt vmcnt(%7)" : "+v"(par[0]), "+v"(par[1]), "+v"(par[2]), "+v"(par[3]), "+v"(par[4]), "+v"(par[5]), "+v"(par[6]) : "i"(EARLY == 2 ? 7 : 0) : "memory");
        STAMP(1);
        const float dt = rbf(__builtin_amdgcn_exp2f(blo(par[6]) * 1.44269504f));
        const float lr2 = dt * blo(par[0]) * 1.44269504f, rev = dt * blo(par[1]) * 0.15915494f;
        const float br = dt * blo(par[2]), bi = dt * blo(par[3]), cr = blo(par[4]), ci = blo(par[5]);
        f2 W = {cr * br - ci * bi, cr * bi + ci * br};
        unsigned mx = max(asu(W.x) & 0x7fffffffu, asu(W.y) & 0x7fffffffu);
        mx = max(mx, asu(dppf<0x128>(asf(mx)))); mx = max(mx, asu(dppf<0x124>(asf(mx)))); mx = max(mx, asu(dppf<0x122>(asf(mx)))); mx = max(mx, asu(dppf<0x121>(asf(mx))));
        const unsigned mu = max(max((unsigned)__builtin_amdgcn_readlane(mx, 0), (unsigned)__builtin_amdgcn_readlane(mx, 16)), max((unsigned)__builtin_amdgcn_readlane(mx, 32), (unsigned)__builtin_amdgcn_readlane(mx, 48)));
        const unsigned ex = min(max(mu >> 23, 30u), 230u);
        const float sc = asf((266u - ex) << 23), isc = asf((ex - 12u) << 23);
        SETM0(j * XY_W);
#define PUT(t, row, v) WTID(pkh(v), L_XY + (t) * XY_T + (row) * 272)
        {
            const f2 y8 = cexpn(lr2, -rev, 8.f), y8p = perp(y8);
            const f2 z1 = cexpn(lr2, rev, 1.f), z1p = perp(z1), z2 = cm(z1, z1, z1p), z2p = perp(z2), z4 = cm(z2, z2, z2p), z4p = perp(z4);
            const f2 zc = cexpn(lr2, rev, 128.f), zcp = perp(zc);
            f2 X = W * sc, y = {1.f, 0.f};
#pragma unroll
            for (int i = 0; i < 16; ++i) {
                const f2 Xn = cm(X, zc, zcp), yn = i == 0 ? y8 : cm(y, y8, y8p);
                PUT(1, i, X); PUT(0, i, y);
                if (i < 8) {
                    f2 X4 = cm(X, z4, z4p); PUT(2, i, X4);
                    if (i < 4) {
                        f2 X2 = cm(X, z2, z2p), X6 = cm(X4, z2, z2p); PUT(2, 8 + 2 * i, X2); PUT(2, 9 + 2 * i, X6);
                        if (i < 2) { PUT(3, 4 * i, cm(X, z1, z1p)); PUT(3, 4 * i + 1, cm(X2, z1, z1p)); PUT(3, 4 * i + 2, cm(X4, z1, z1p)); PUT(3, 4 * i + 3, cm(X6, z1, z1p)); }
                    }
                }
                X = Xn; y = yn;
            }
            PUT(3, 8, X);
        }
        STAMP(14);
        SB();
        {
            const unsigned fa = L_XY + j * XY_W + r16 * 272 + q * 16;
            v4u fr[4][4];
#pragma unroll
            for (int s = 0; s < 4; ++s)
#pragma unroll
                for (int t = 0; t < 4; ++t) fr[s][t] = L4(fa + t * XY_T + s * 64);
            SB();
            f4 d[3] = {{0, 0, 0, 0}, {0, 0, 0, 0}, {0, 0, 0, 0}};
#pragma unroll
            for (int s = 0; s < 4; ++s)
#pragma unroll
                for (int t = 0; t < 3; ++t) d[t] = mm16h(fr[s][t + 1], fr[s][0], d[t]);
#pragma unroll
            for (int t = 0; t < 3; ++t)
#pragma unroll
                for (int e = 0; e < 4; ++e) LF1(wa_[t * 4 + e]) = d[t][e] * isc;
        }
        sig(lds, F_KP0 + ch);
        if constexpr (EARLY == 1) {
#pragma unroll
            for (int i = 0; i < 7; ++i) W1P(i);
        }
        wt(lds, F_KP0 + ch, 2);
        STAMP(2);
        const unsigned kib = L_XY + ch * KI_CH;
        {
            f2 kp[19]; float kv[19];
#pragma unroll
            for (int e = 0; e < 19; ++e) kp[e] = LF2(ta[e]);
            SB();
#pragma unroll
            for (int e = 0; e < 19; ++e) kv[e] = kp[e].x + kp[e].y;
            const unsigned ka = kib + (hf * 64 + lane) * 32;
#pragma unroll
            for (int c = 0; c < 4; ++c)
#pragma unroll
                for (int hx = 0; hx < 2; ++hx) {
                    const int e = c + 8 * hx;
                    v4u pk_;
#pragma unroll
                    for (int i = 0; i < 4; ++i) { unsigned t_ = pkb(kv[e + 2 * i], kv[e + 2 * i + 1]); asm volatile("" : "+v"(t_)); pk_[i] = t_; }
                    L4(ka + c * KI_C + hx * 16) = pk_;
                }
            if (lane < 4) L4(kib + lane * KI_C + 4096 + hf * 16) = (v4u){0, 0, 0, 0};
        }
        sig(lds, F_KE0 + ch); wt(lds, F_KE0 + ch, 2);
        STAMP(15);
        v4u af[4][8];
        {
            const unsigned aa = kib + ((3 - r16) & 3) * KI_C + 2 * (2028 - 4 * (r16 >> 2) + 8 * q - 128 * hf - 32 * 59);
#pragma unroll
            for (int d = 0; d < 4; ++d)
#pragma unroll
                for (int t = 0; t < 8; ++t) { const unsigned o = aa + 64 * (59 - d - 8 * t);
                    v2u p0 = L2V(o), p1 = L2V(o + 8); af[d][t] = (v4u){p0.x, p0.y, p1.x, p1.y}; }
        }
        sig(lds, F_AF);
        b1v = *(const v2u*)(a.b1 + mem1 * 112 + j * 16 + q * 4);
        b1w = *(const v2u*)(a.b1 + mem1 * 112 + (j < 3 ? j + 4 : j) * 16 + q * 4);
        STAMP(3);
        wt(lds, F_H, 4);
        STAMP(4);
        __builtin_amdgcn_s_setprio(1);
        f4 bs[2][2];
        v4u mv;
        f4 acc[8];
#pragma unroll
        for (int c = 0; c < 8; ++c) acc[c] = (f4){0, 0, 0, 0};
        {
            const unsigned ha = L_H + ch * H_CH + 2 * (240 + 16 * r16 + 8 * q - 128 * hf - 96);
            unsigned ba = L_EX + (512 * j + 16 * r16 + 4 * q) * 4, ea = L_EX + j * 8192 + lane * 16;
            asm volatile("" : "+v"(ba), "+v"(ea));
            v4u bf[2][8];
#pragma unroll
            for (int e = 0; e < 8; ++e) { bf[0][e] = L4(ha + 64 * (8 * e + 3)); SB(); }
#pragma unroll
            for (int d = 0; d < 3; ++d) {
                int n = 0;
                if (d == 2) { if constexpr (EARLY) { VM(0); sig(lds, F_W1); } wt(lds, F_AF, 8); }
#pragma unroll
                for (int e = 0; e < 8; ++e)
#pragma unroll
                    for (int t = 0; t + e < 8; ++t) {
                        acc[t + e] = mm16(af[d][t], bf[d & 1][e], acc[t + e]);
                        if ((n & 3) == 1 && n < 32) bf[(d + 1) & 1][n >> 2] = L4(ha + 64 * (8 * (n >> 2) + 3 - (d + 1)));
                        if (d == 2 && n >= 2 && n < 5 + 1 && n != 5) bs[(n - 2) >> 1][n & 1] = LF4(ba + ((n - 2) >> 1) * 8192 + (n & 1) * 1024);
                        if (d == 2 && n == 6) bs[1][1] = LF4(ba + 8192 + 1024);
                        if (d == 2 && n == 7) mv = L4(L_MV);
                        if (d == 2 && n == 15) sig(lds, F_BASE);
                        ++n;
                        SB();
                    }
                if (d == 2) wt(lds, F_BASE, 4);
            }
#pragma unroll
            for (int c = 7; c >= 0; --c) {
#pragma unroll
                for (int t = 0; t <= c; ++t) {
                    acc[c] = mm16(af[3][t], bf[1][c - t], acc[c]);
                    if (c < 7 && t == (c < 2 ? c : 2)) LF4(ea + (c + 1) * 1024) = acc[c + 1];
                    SB();
                }
            }
            LF4(ea) = acc[0];
        }
        __builtin_amdgcn_s_setprio(0);
        STAMP(5);
        sig(lds, F_EX); wt(lds, F_EX, 4);
        tag = mv.z;
        {
            const float ga0 = blo(mv.x), ga1 = bhi(mv.x), c10 = blo(mv.y), c11 = bhi(mv.y);
            const unsigned so = ((lane >> 3) & 1) * 131072 + q * 128 + (lane & 7) * 16;
            f4 p[2][4];
#pragma unroll
            for (int t = 0; t < 2; ++t)
#pragma unroll
                for (int i = 0; i < 4; ++i) p[t][i] = LF4(L_EX + i * 8192 + (2 * j + t) * 1024 + lane * 16);
            SB();
#pragma unroll
            for (int t = 0; t < 2; ++t) {
                f4 u0 = __builtin_elementwise_fma(p[t][0] + p[t][1], (f4){ga0, ga0, ga0, ga0}, bs[0][t]), u1 = __builtin_elementwise_fma(p[t][2] + p[t][3], (f4){ga1, ga1, ga1, ga1}, bs[1][t]);
                asm volatile("" : "+v"(u0), "+v"(u1));
                f4 s0 = u0 * c10, s1 = u1 * c11;
                asm volatile("" : "+v"(s0), "+v"(s1));
                v4u xo, zo;
#pragma unroll
                for (int e = 0; e < 4; ++e) { xo[e] = pkb(u0[e], u1[e]); zo[e] = pkb(s0[e], s1[e]); }
                ST4(zo, rw, so, O_Z + (4 * j + 2 * t) * 131072 + b * 512);
                ST4(xo, rw, so, O_X1 + (4 * j + 2 * t) * 131072 + b * 512);
            }
        }
        STAMP(6);
        VM(0);
        if (sigr(lds, F_ZD) == 3) { if (lane == 0) ST1(tag, rw, 0, O_F2 + b * 128); }
        STAMP(7);
    }
    {
        {
            wt(lds, F_W1, EARLY ? 8 : 4);
            f4 d0 = {0, 0, 0, 0}, d1 = {0, 0, 0, 0};
            unsigned wa = (4 * q + (r16 >> 2)) * 224 + (r16 & 3) * 8 + j * 32, wb = wa + (j < 3 ? 128 : 0);
            unsigned ca = L_CS + (r16 & 1) * CS_B + q * 8;
#pragma unroll 1
            for (int g = 0; g < 2; ++g) {
                v4u bs[8], a0[8], a1[8];
#pragma unroll
                for (int u = 0; u < 8; ++u) {
                    v2u c0 = L2V(ca + u * 64), c1 = L2V(ca + u * 64 + 32);
                    bs[u] = (v4u){c0.x, c0.y, c1.x, c1.y};
                    a0[u] = trd2(lds, wa + u * 7168, wa + u * 7168 + 3584); a1[u] = trd2(lds, wb + u * 7168, wb + u * 7168 + 3584);
                    SB();
                    if (u >= 2) { d0 = mm16(a0[u - 2], bs[u - 2], d0); d1 = mm16(a1[u - 2], bs[u - 2], d1); }
                    SB();
                }
#pragma unroll
                for (int u = 6; u < 8; ++u) { d0 = mm16(a0[u], bs[u], d0); d1 = mm16(a1[u], bs[u], d1); }
                wa += 57344; wb += 57344; ca += 512;
            }
            if (r16 == 1) {
                d0 += (f4){blo(b1v.x), bhi(b1v.x), blo(b1v.y), bhi(b1v.y)};
                d1 += (f4){blo(b1w.x), bhi(b1w.x), blo(b1w.y), bhi(b1w.y)};
            }
            if (r16 < 2) {
#pragma unroll
                for (int e = 0; e < 4; ++e) {
                    float h = rbf(d0[e]); L1(L_AP + (j * 16 + q * 4 + e) * 8 + r16 * 4) = pkb(h, d0[e] - h);
                    if (j < 3) { float g = rbf(d1[e]); L1(L_AP + ((j + 4) * 16 + q * 4 + e) * 8 + r16 * 4) = pkb(g, d1[e] - g); }
                }
            }
            if (lane < 2) L1(L_ZR + lane * 4) = 0;
            STAMP(13);
        }
        {
            bool ok = false;
            if (P2A) __builtin_amdgcn_s_sleep(P2A);
            for (unsigned n = 0; n < SPIN; ++n) {
                CFENCE();
                if (!ok) ok = LD1(rw, lane * 128, O_F2 + j * 8192) == tag;
                if (__all(ok)) break;
                __builtin_amdgcn_s_sleep(P2B);
            }
            CFENCE();
            sig(lds, F_E2);
            wt(lds, F_E2, 4);
        }
        STAMP(8);
        f4 c1[14];
#pragma unroll
        for (int i = 0; i < 14; ++i) c1[i] = (f4){0, 0, 0, 0};
        {
            const unsigned zo = (lane >> 3) * 512 + (lane & 7) * 16, zo2 = zo + 4096, zb = O_Z + team1 * 131072 + j * 128;
            unsigned wa = (4 * q + (r16 >> 2)) * 224 + (r16 & 3) * 8;
            v4u z[4][2];
            v2u wv[2][14];
#define ZL1(sl, i, so) z[sl][i] = LD4(rw, (i) ? zo2 : zo, so)
#define XCH(d0, d1, m, a0, b0, a1, b1) asm volatile("s_mov_b64 vcc, %2\n\tv_cndmask_b32_dpp %0, %3, %4, vcc row_ror:8 row_mask:0xf bank_mask:0xf\n\tv_cndmask_b32_dpp %1, %5, %6, vcc row_ror:8 row_mask:0xf bank_mask:0xf" \
                : "=&v"(d0), "=&v"(d1) : "s"(m), "v"(a0), "v"(b0), "v"(a1), "v"(b1) : "vcc")
            const unsigned long long mlo = 0x00ff00ff00ff00ffull, mhi = ~mlo;
#define XPART(k, zz, A, B) do { unsigned x_, y_; if ((k) & 1) XCH(x_, y_, mhi, zz.x, zz.z, zz.y, zz.w); else XCH(x_, y_, mlo, zz.z, zz.x, zz.w, zz.y); A[k] = x_; B[k] = y_; } while (0)
            unsigned g0 = zb + (rot & 3) * 32768;
#pragma unroll
            for (int u = 0; u < 4; ++u)
#pragma unroll
                for (int i = 0; i < 2; ++i) { ZL1(u, i, g0 + u * 8192); SB(); }
            g0 = zb + ((rot + 1) & 3) * 32768;
            __builtin_amdgcn_s_setprio(1);
#pragma unroll
            for (int t = 0; t < 14; ++t) wv[0][t] = trd(lds, wa + (t >> 1) * 32 + (t & 1) * 3584);
            v4u fa, fb;
#pragma unroll
            for (int k = 0; k < 4; ++k) XPART(k, z[0][k >> 1], fa, fb);
            SB(); ZL1(0, 0, g0); SB(); ZL1(0, 1, g0); SB();
            __builtin_amdgcn_s_waitcnt(0xc07f);
            STAMP(9);
            auto body = [&](auto last_, unsigned gc, unsigned gn) {
                constexpr bool last = decltype(last_)::value;
#pragma unroll
                for (int u = 0; u < 4; ++u) {
                    v4u na, nb;
                    const int s1 = (u + 1) & 3;
#pragma unroll
                    for (int t = 0; t < 14; ++t) {
                        const v2u w0 = wv[u & 1][t & ~1], w1_ = wv[u & 1][t | 1];
                        c1[t] = mm16((v4u){w0.x, w0.y, w1_.x, w1_.y}, (t & 1) ? fb : fa, c1[t]);
                        SB();
                        if (!(last && u == 3)) {
                            if (t < 7) {
                                wv[(u + 1) & 1][2 * t] = trd(lds, wa + (u + 1) * 7168 + t * 32);
                                wv[(u + 1) & 1][2 * t + 1] = trd(lds, wa + (u + 1) * 7168 + t * 32 + 3584);
                            }
                            if (t >= 7 && t < 11) XPART(t - 7, z[s1][(t - 7) >> 1], na, nb);
                            if (t == 12) __builtin_amdgcn_s_waitcnt(0xc07f);
                        }
                        if ((t == 11 || t == 13) && !last) ZL1(s1, (t - 11) >> 1, u < 3 ? gc + (u + 1) * 8192 : gn);
                        if (last && u == 3 && t == 6) sig(lds, F_RD);
                        SB();
                    }
                    fa = na; fb = nb;
                }
            };
#pragma unroll 1
            for (int it = 0; it < 3; ++it) {
                const unsigned g1 = zb + ((it + 2 + rot) & 3) * 32768;
                body(std::false_type{}, g0, g1);
                g0 = g1; wa += 28672;
            }
            body(std::true_type{}, g0, g0);
            asm volatile("" :: "v"(z[0][0]), "v"(z[0][1]));
        }
        STAMP(10);
        wt(lds, F_ST, 4);
        {
            const v2u bA = L2(q ? L_ZR : L_BT + (j * 32 + r16) * 8), bB = L2(q ? L_ZR : L_BT + (j * 32 + 16 + r16) * 8);
            const float rA = LF1(L_R + (j * 32 + r16) * 4), rB = LF1(L_R + (j * 32 + 16 + r16) * 4);
            const unsigned hb = O_H + team1 * 458752 + j * 114688 + mem1 * 7168;
#pragma unroll
            for (int t = 0; t < 7; ++t) {
                const v2u ap = L2(L_AP + (t * 16 + r16) * 8);
                f4 uA = mm16s(ap, bA, c1[2 * t]) * rA, uB = mm16s(ap, bB, c1[2 * t + 1]) * rB;
#pragma unroll
                for (int e = 0; e < 4; ++e) { uA[e] = __builtin_fmaxf(uA[e], 0.f); uB[e] = __builtin_fmaxf(uB[e], 0.f); }
                uA *= uA; uB *= uB;
                v2u s0 = __builtin_amdgcn_permlane16_swap(pkb(uA[0], uA[1]), pkb(uB[0], uB[1]), false, false);
                v2u s1 = __builtin_amdgcn_permlane16_swap(pkb(uA[2], uA[3]), pkb(uB[2], uB[3]), false, false);
                ST4(((v4u){s0.x, s1.x, s0.y, s1.y}), rw, lane * 16, hb + t * 1024);
            }
        }
        STAMP(11);
        VM(0);
        if (lane == 0) ST1(tag, rw, 0, O_F3 + ((team1 * 16 + mem1) * 4 + j) * 128);
        STAMP(12);
    }
    STAMP_OUT(0);
}

extern "C" void kernel_launch(void* const* d_in, const int* in_sizes, int n_in, void* d_out, int out_size, void* d_ws, size_t ws_size, hipStream_t stream) {
    Args a;
    a.x = (const u16*)d_in[0]; a.Are = (const u16*)d_in[1]; a.Aim = (const u16*)d_in[2]; a.Bre = (const u16*)d_in[3]; a.Bim = (const u16*)d_in[4];
    a.Cre = (const u16*)d_in[5]; a.Cim = (const u16*)d_in[6]; a.ldt = (const u16*)d_in[7]; a.Dsk = (const u16*)d_in[8];
    a.w1 = (const u16*)d_in[10]; a.b1 = (const u16*)d_in[11]; a.w2 = (const u16*)d_in[12]; a.b2 = (const u16*)d_in[13];
    a.c = (const u16*)d_in[14]; a.wm = (const u16*)d_in[15];
    a.out = (u16*)d_out; a.ws = (char*)d_ws + (WSEND ? ((ws_size - WSB) & ~(size_t)0x1fffff) : 0); a.st = (unsigned long long*)((char*)d_ws + (32u << 20));
    (void)hipFuncSetAttribute((const void*)fused, hipFuncAttributeMaxDynamicSharedMemorySize, LDS_BYTES);
    fused<<<256, 512, LDS_BYTES, stream>>>(a);
}
```

```cpp
#include <hip/hip_runtime.h>
#include <stdint.h>
#include <type_traits>

typedef unsigned short u16;
typedef unsigned v4u __attribute__((ext_vector_type(4)));
typedef unsigned v2u __attribute__((ext_vector_type(2)));
typedef float f2 __attribute__((ext_vector_type(2)));
typedef float f4 __attribute__((ext_vector_type(4)));
typedef float f16v __attribute__((ext_vector_type(16)));
typedef short s4 __attribute__((ext_vector_type(4)));
typedef __bf16 bf2 __attribute__((ext_vector_type(2)));
typedef __bf16 bf8 __attribute__((ext_vector_type(8)));
typedef _Float16 h2 __attribute__((ext_vector_type(2)));
typedef _Float16 h8 __attribute__((ext_vector_type(8)));
typedef __amdgpu_buffer_rsrc_t rsrc_t;
#define LAS __attribute__((address_space(3)))
#define DEV __device__ __forceinline__

#define EARLY 0
#define CPF 1
#define ZWARM 1
#define PDLY 0
#define P2A 10
#define P2B 3
#define MEARLY 24
#define GP 2
#define E1MODE 1
#define PSD 10
#define WSEND 0
#define RINGE 0
#define NT 0
#define NTS ""
#define WMODE 0
#define PADP 0
#define PAD(i) do { if (PADP == (i)) { while (__builtin_amdgcn_s_memrealtime() < tpad) {} } } while (0)
#define HWIN 5

struct Args {
    const u16 *x, *Are, *Aim, *Bre, *Bim, *Cre, *Cim, *ldt, *Dsk, *w1, *b1, *w2, *b2, *c, *wm;
    u16* out; char* ws; unsigned long long* st;
};

constexpr unsigned O_E1 = 0, O_ST = 4096, O_MD = 65536, O_F2 = 196608, O_F3 = 262144, O_PS = 524288;
constexpr unsigned O_Z = 4u << 20, O_X1 = 6u << 20, O_H = 8u << 20, O_XG = 16u << 20, WSB = 20u << 20;
constexpr unsigned L_XY = 28672, XY_W = 15504, XY_T = 4352, KI_C = 4160, KI_CH = 2 * XY_W, L_MS = L_XY + 4 * XY_W, L_SI = L_MS + 16384, L_T0 = L_SI + 512;
constexpr unsigned L_H = 114688, H_CH = 4608, L_EX = 123904, L_XS = L_EX + 2048, L_T1 = L_EX + 16384 + 2048;
constexpr unsigned L_MV = 156672, L_CS = L_MV + 128, CS_B = 1152, L_GB = L_CS + 2304, L_AP = L_GB + 128, L_ZR = L_AP + 896, L_BT = L_AP + 1024, L_R = L_BT + 1024, L_SP = L_R + 512, L_JK = L_SP + 64, L_PJ = L_JK;
constexpr unsigned L_FLG = 163840 - 128, LDS_BYTES = 163840;
static_assert(L_JK + 1024 <= L_FLG, "lds");
constexpr unsigned W2_W = 38912;
constexpr int NA = 8, NR = 18;
enum { F_XTD = 0, F_KP0, F_KP1, F_KE0, F_KE1, F_AF, F_H, F_EX, F_ZD, F_E2, F_W1, F_ST, F_RD, F_BASE, F_MJ, F_PJ };
constexpr unsigned SPIN = 1u << 17;

DEV float asf(unsigned v) { return __uint_as_float(v); }
DEV unsigned asu(float v) { return __float_as_uint(v); }
DEV float blo(unsigned v) { return asf(v << 16); }
DEV float bhi(unsigned v) { return asf(v & 0xffff0000u); }
DEV unsigned pkb(float a, float b) { bf2 r = __builtin_convertvector((f2){a, b}, bf2); return __builtin_bit_cast(unsigned, r); }
DEV unsigned pkh(f2 a) { h2 r = __builtin_convertvector(a, h2); return __builtin_bit_cast(unsigned, r); }
DEV float rbf(float a) { return blo(pkb(a, a)); }
DEV float ad1(float a, float b) { float r; asm("v_add_f32 %0, %1, %2" : "=v"(r) : "v"(a), "v"(b)); return r; }
DEV float mu1(float a, float b) { float r; asm("v_mul_f32 %0, %1, %2" : "=v"(r) : "v"(a), "v"(b)); return r; }
DEV float fm1(float a, float b, float c) { float r; asm("v_fma_f32 %0, %1, %2, %3" : "=v"(r) : "v"(a), "v"(b), "v"(c)); return r; }
DEV unsigned rfl(unsigned v) { return __builtin_amdgcn_readfirstlane(v); }
template <int C> DEV float dppf(float v) { return asf((unsigned)__builtin_amdgcn_update_dpp(0, (int)asu(v), C, 0xf, 0xf, true)); }
DEV float red16(float v) { v += dppf<0x128>(v); v += dppf<0x124>(v); v += dppf<0x122>(v); v += dppf<0x121>(v); return v; }
DEV float sw32add(float a, float b) { v2u r = __builtin_amdgcn_permlane32_swap(asu(a), asu(b), false, false); unsigned x = r.x, y = r.y; return ad1(asf(x), asf(y)); }
DEV float sw16add(float a, float b) { v2u r = __builtin_amdgcn_permlane16_swap(asu(a), asu(b), false, false); unsigned x = r.x, y = r.y; return ad1(asf(x), asf(y)); }
DEV rsrc_t mk(const void* p, unsigned n) { return __builtin_amdgcn_make_buffer_rsrc((void*)p, 0, n, 0x00020000); }
DEV void dma(rsrc_t r, unsigned voff, unsigned soff, unsigned ldst) {
    asm volatile("s_mov_b32 m0, %0\n\ts_nop 0\n\tbuffer_load_dwordx4 %1, %2, %3 offen lds" :: "s"(ldst), "v"(voff), "s"(r), "s"(soff) : "memory");
}
DEV void dma1(rsrc_t r, unsigned voff, unsigned soff, unsigned ldst) {
    asm volatile("s_mov_b32 m0, %0\n\ts_nop 0\n\tbuffer_load_dwordx4 %1, %2, %3 offen" NTS " lds" :: "s"(ldst), "v"(voff), "s"(r), "s"(soff) : "memory");
}
#define SETM0(v) asm volatile("s_mov_b32 m0, %0\n\ts_nop 0" :: "s"(v) : "memory")
#define WTID(v, off) asm volatile("ds_write_addtid_b32 %0 offset:%1" :: "v"(v), "i"(off) : "memory")
#define SB() __builtin_amdgcn_sched_barrier(0)
#define VM(n) asm volatile("s_waitcnt vmcnt(%0)" :: "i"(n) : "memory")
#define LG0() asm volatile("s_waitcnt lgkmcnt(0)" ::: "memory")
#define CFENCE() asm volatile("" ::: "memory")
#define LD4(r, vo, so) __builtin_amdgcn_raw_buffer_load_b128(r, vo, so, 16)
#define LD2(r, vo, so) __builtin_amdgcn_raw_buffer_load_b64(r, vo, so, 16)
#define LD1(r, vo, so) __builtin_amdgcn_raw_buffer_load_b32(r, vo, so, 16)
#define ST4(v, r, vo, so) __builtin_amdgcn_raw_buffer_store_b128(v, r, (vo) + (so), 0, 16)
#define ST1(v, r, vo, so) __builtin_amdgcn_raw_buffer_store_b32(v, r, vo, so, 16)
#define L4(a) (*(LAS v4u*)(lds + (a)))
#define L2(a) (*(LAS v2u*)(lds + (a)))
#define L2V(a) (*(volatile LAS v2u*)(lds + (a)))
#define L1(a) (*(LAS unsigned*)(lds + (a)))
#define LF4(a) (*(LAS f4*)(lds + (a)))
#define LF2(a) (*(LAS f2*)(lds + (a)))
#define LF1(a) (*(LAS float*)(lds + (a)))
typedef LAS char* lptr;
DEV v2u trd(lptr lds, unsigned a) { s4 v = __builtin_amdgcn_ds_read_tr16_b64_v4i16((LAS s4*)(lds + a)); return __builtin_bit_cast(v2u, v); }
DEV v4u trd2(lptr lds, unsigned a, unsigned b) { v2u p = trd(lds, a), q = trd(lds, b); return (v4u){p.x, p.y, q.x, q.y}; }
DEV f4 mm16(v4u a, v4u b, f4 c) { return __builtin_amdgcn_mfma_f32_16x16x32_bf16(__builtin_bit_cast(bf8, a), __builtin_bit_cast(bf8, b), c, 0, 0, 0); }
DEV f4 mm16h(v4u a, v4u b, f4 c) { return __builtin_amdgcn_mfma_f32_16x16x32_f16(__builtin_bit_cast(h8, a), __builtin_bit_cast(h8, b), c, 0, 0, 0); }
DEV f4 mm16s(v2u a, v2u b, f4 c) { return __builtin_amdgcn_mfma_f32_16x16x16bf16_1k(__builtin_bit_cast(s4, a), __builtin_bit_cast(s4, b), c, 0, 0, 0); }
DEV f16v mm32(v4u a, v4u b, f16v c) { return __builtin_amdgcn_mfma_f32_32x32x16_bf16(__builtin_bit_cast(bf8, a), __builtin_bit_cast(bf8, b), c, 0, 0, 0); }

DEV void sig(lptr lds, int i) {
    const unsigned ad = (unsigned)(size_t)lds + L_FLG + i * 4, one = 1;
    asm volatile("s_waitcnt lgkmcnt(0)\n\ts_mov_b64 exec, 1\n\tds_add_u32 %0, %1\n\ts_mov_b64 exec, -1" :: "v"(ad), "v"(one) : "memory");
}
DEV unsigned sigr(lptr lds, int i) {
    const unsigned ad = (unsigned)(size_t)lds + L_FLG + i * 4, one = 1; unsigned o;
    asm volatile("s_waitcnt lgkmcnt(0)\n\ts_mov_b64 exec, 1\n\tds_add_rtn_u32 %0, %1, %2\n\ts_mov_b64 exec, -1\n\ts_waitcnt lgkmcnt(0)" : "=&v"(o) : "v"(ad), "v"(one) : "memory");
    return rfl(o);
}
template <int SLEEP> DEV void wts(lptr lds, int i, unsigned n) {
    _Pragma("unroll 1") for (unsigned s = 0; s < (SPIN << 6); ++s) {
        unsigned v = __hip_atomic_load((LAS unsigned*)(lds + L_FLG) + i, __ATOMIC_RELAXED, __HIP_MEMORY_SCOPE_WORKGROUP);
        if (rfl(v) >= n) break;
        if (SLEEP) __builtin_amdgcn_s_sleep(SLEEP);
    }
    CFENCE();
}
#define wt wts<0>
#define wtl wts<1>
DEV f2 cm(f2 a, f2 m, f2 mp) { f2 t = m * (f2){a.x, a.x}; return __builtin_elementwise_fma(mp, (f2){a.y, a.y}, t); }
DEV f2 perp(f2 m) { return (f2){-m.y, m.x}; }
DEV f2 cexpn(float lr2, float rev, float p) {
    float mg = __builtin_amdgcn_exp2f(lr2 * p), ph = __builtin_amdgcn_fractf(rev * p);
    return (f2){mg * __builtin_amdgcn_cosf(ph), mg * __builtin_amdgcn_sinf(ph)};
}

#define STAMP(i) do {} while (0)
#define STAMP_DECL
#define STAMP_OUT(base) do {} while (0)

#define W1P(i) dma(rw1, vo1[(i) % 7], ((((i) / 7 + rot) & 3) * 4 + j) * (32 * 3584), (((i) / 7 * 4 + j) * 7 + (i) % 7) * 1024)
#define VO1() _Pragma("unroll") for (int i = 0; i < 7; ++i) { int c = i * 64 + lane; vo1[i] = (c / 14) * (3584 - 224) + c * 16 + mem1 * 224; }

__global__ void __launch_bounds__(512, 2) fused(Args a) {
    extern __shared__ __attribute__((aligned(16))) char smem[];
    lptr lds = (lptr)smem;
    const int w = rfl(threadIdx.x >> 6), lane = threadIdx.x & 63, j = w & 3;
    const int xcd = blockIdx.x & 7, ix = blockIdx.x >> 3, b = xcd * 32 + ix;
    const int team1 = (xcd >> 1) * 4 + (ix >> 3), mem1 = (xcd & 1) * 8 + (ix & 7);
    const int team2 = team1, th = (ix >> 2) & 1, cb2 = (xcd & 1) * 4 + (ix & 3), rot = ix >> 3, rk = ((ix >> 2) * 7) >> 1;
    const int q = lane >> 4, r16 = lane & 15;
    const rsrc_t rw = mk(a.ws, WSB);
    STAMP_DECL
    const unsigned long long tpad = PADP ? __builtin_amdgcn_s_memrealtime() + 3000 : 0;
    STAMP(0);
    {
        unsigned long long pc; unsigned len;
        asm volatile(".Lkbeg:\n\ts_getpc_b64 %0\n\ts_mov_b32 %1, .Lkend-.Lkbeg" : "=s"(pc), "=s"(len));
        if (w < 2) dma(mk((const void*)pc, len), lane * 16, (w * 32 + ix) * 1024, L_JK);
    }

    if (w >= 4) {
        unsigned tag, ep;
        __builtin_amdgcn_s_setprio(2);
        asm volatile("buffer_load_dword %0, off, %1, 0 sc1" : "=v"(ep) : "s"(rw) : "memory");
        const rsrc_t rw1 = mk(a.w1, 512 * 1792 * 2), rw2 = mk(a.w2, 512 * 1792 * 2);
        unsigned vo1[7];
        v4u wf[NR];
        const unsigned w2vo = j * 458752 + (lane >> 3) * 1024 + cb2 * 128 + (((lane & 7) ^ (((lane >> 4) & 1) << 2)) * 16);
        unsigned w2o = rk * 16384;
        auto w2run = [&](int n, unsigned dst) {
#pragma unroll 1
            for (int i = 0; i < n; i += 2) {
                dma(rw2, w2vo, w2o, dst); dma(rw2, w2vo, w2o + 8192, dst + 1024);
                dst += 2048; w2o += 16384; if (w2o == 458752) w2o = 0;
            }
        };
#define W2R(i, src) do { wf[i] = trd2(lds, (src) + w2ra, (src) + w2ra + 512); wf[(i) + 1] = trd2(lds, (src) + (w2ra ^ 64), (src) + (w2ra ^ 64) + 512); } while (0)
        v4u xa[4]; unsigned cv = 0;
        const bool domod = ix < 24;
        const int kq = (ix & 1) * 2 + (j & 1), mcb = ((ix >> 2) * 8 + xcd) * 2 + ((ix >> 1) & 1);
        {
            const rsrc_t rx = mk(a.x, 2048 * 512 * 2);
            if (j < 2) {
#pragma unroll
                for (int i = 0; i < 4; ++i) dma1(rx, q * 1024 + ((r16 ^ (2 * i + (q >> 1))) * 16), ((b >> 1) * 16 + 4 * i) * 1024 + (b & 1) * 512 + j * 256, L_XS + j * 8192 + i * 1024);
            } else {
                asm volatile("global_load_dword %0, %1, off" : "=v"(cv) : "v"(a.c + kq * 128 + lane * 2) : "memory");
#pragma unroll
                for (int i = 0; i < 4; ++i) asm volatile("buffer_load_dwordx4 %0, %1, %2, %3 offen offset:%4" NTS : "=v"(xa[i]) : "v"(q * 1024 + r16 * 16), "s"(rx), "s"((b * 8 + (j & 1) * 4) * 1024), "i"(i * 256) : "memory");
                const rsrc_t rm = mk(a.wm, ix < MEARLY ? 512 * 3072 * 2 : 0);
#pragma unroll
                for (int i = 0; i < 8; ++i) dma1(rm, (lane >> 2) * 6144 + (lane & 3) * 16, (kq * 128 + i * 16) * 6144 + mcb * 64, L_MS + (j & 1) * 8192 + i * 1024);
            }
        }
        __builtin_amdgcn_s_barrier();
        if (j < 2) {
            asm volatile("s_waitcnt vmcnt(0)\n\tv_add_u32 %0, 1, %1" : "=v"(tag) : "v"(ep) : "memory");
            v4u t4[4];
#pragma unroll
            for (int i = 0; i < 4; ++i) t4[i] = L4(L_XS + j * 8192 + (2 * (lane & 7) + (i & 1)) * 256 + ((((lane >> 3) + 8 * (i >> 1)) ^ (lane & 7)) * 16));
#pragma unroll
            for (int i = 0; i < 8; ++i) { v4u g = {t4[(i >> 2) * 2][i & 3], tag, t4[(i >> 2) * 2 + 1][i & 3], tag};
                ST4(g, rw, ((lane >> 3) * 4 + (i & 3) + (i >> 2) * 32) * 16384 + (lane & 7) * 16, O_XG + ((b & 1) * 128 + j * 64) * 16384 + (b >> 1) * 128); }
            STAMP(1);
        } else {
            asm volatile("s_waitcnt vmcnt(8)\n\tv_add_u32 %0, 1, %6" : "=v"(tag), "+v"(cv), "+v"(xa[0]), "+v"(xa[1]), "+v"(xa[2]), "+v"(xa[3]) : "v"(ep) : "memory");
            STAMP(1);
            if (ix >= MEARLY && domod) {
                const rsrc_t rm = mk(a.wm, 512 * 3072 * 2);
#pragma unroll
                for (int i = 0; i < 8; ++i) dma1(rm, (lane >> 2) * 6144 + (lane & 3) * 16, (kq * 128 + i * 16) * 6144 + mcb * 64, L_MS + (j & 1) * 8192 + i * 1024);
            }
            f2 v[16]; f2 s2 = {0.f, 0.f};
#pragma unroll
            for (int i = 0; i < 4; ++i)
#pragma unroll
                for (int e = 0; e < 4; ++e) { unsigned u = xa[i][e]; v[i * 4 + e] = (f2){blo(u), bhi(u)}; s2 += v[i * 4 + e]; }
            const float mean = red16(s2.x + s2.y) * (1.f / 512.f);
            f2 q2 = {0.f, 0.f};
#pragma unroll
            for (int i = 0; i < 16; ++i) { f2 dd = v[i] - mean; q2 = __builtin_elementwise_fma(dd, dd, q2); }
            const float var = rbf(red16(q2.x + q2.y) * (1.f / 512.f));
            const float den = __builtin_amdgcn_sqrtf(rbf(var + 1e-5f));
            if (r16 == 0) L1(L_SP + ((j & 1) * 4 + q) * 4) = pkb(mean, den);
            const bool pubd = sigr(lds, F_XTD) == 1;
            if (pubd) {
                v2u p = L2(L_SP + (lane & 3) * 8);
                if (lane < 4) { v4u g = {p.x, tag, p.y, tag}; ST4(g, rw, lane * 16, O_ST + b * 128); }
            }
            float c0 = blo(cv), c1 = bhi(cv);
            float s0 = c0 * __builtin_amdgcn_rcpf(1.f + __builtin_amdgcn_exp2f(-1.44269504f * c0));
            float s1 = c1 * __builtin_amdgcn_rcpf(1.f + __builtin_amdgcn_exp2f(-1.44269504f * c1));
            L1(L_SI + (j & 1) * 256 + lane * 4) = pkb(s0, s1);
            v4u bs[4];
#pragma unroll
            for (int s = 0; s < 4; ++s) bs[s] = L4(L_SI + (j & 1) * 256 + s * 64 + q * 16);
            const unsigned ma = L_MS + (j & 1) * 8192 + (8 * q + (r16 >> 2)) * 64 + (r16 & 3) * 8;
            if (pubd) VM(1); else VM(0);
            SB();
            STAMP(15);
            v4u am[8];
#pragma unroll
            for (int s = 0; s < 8; ++s) { am[s] = trd2(lds, ma + (s >> 1) * 2048 + (s & 1) * 32, ma + (s >> 1) * 2048 + (s & 1) * 32 + 256); }
            SB();
            f4 d0 = {0, 0, 0, 0}, d1 = {0, 0, 0, 0};
#pragma unroll
            for (int s = 0; s < 4; ++s) { d0 = mm16(am[2 * s], bs[s], d0); d1 = mm16(am[2 * s + 1], bs[s], d1); }
            f4 d = (r16 & 2) ? d1 : d0;
            f2 e = {(r16 & 1) ? d[2] : d[0], (r16 & 1) ? d[3] : d[1]};
            LF2(L_MS + (j & 1) * 8192 + lane * 8) = e;
            if (sigr(lds, F_MJ) == 1) {
                e += LF2(L_MS + ((j & 1) ^ 1) * 8192 + lane * 8);
                if (domod && r16 < 4) { v4u g = {asu(e.x), tag, asu(e.y), tag};
                    ST4(g, rw, ((r16 >> 1) * 16 + q * 4 + (r16 & 1) * 2) * 8, O_MD + (ix & 1) * 24576 + mcb * 256); }
            }
            STAMP(2);
        }
        SB();
        const unsigned w2ra = (q >> 1) * 1024 + (r16 >> 2) * 128 + (((2 * (q & 1) + ((r16 & 3) >> 1)) ^ ((r16 >> 3) << 2)) * 16) + 8 * (r16 & 1);
        VO1();
        {
            v4u s[4], xg[4], md;
            const unsigned mo = (lane & 1) * 24576 + (min(lane >> 2, 4) * 512 + 2 * b) * 8;
            if (j < 2) wtl(lds, F_XTD, 2);
            if (PDLY && j < 2) __builtin_amdgcn_s_sleep(PDLY);
            bool okm[4] = {false, false, false, false}, okd = false;
            const bool gate = E1MODE == 1 || (E1MODE == 2 && j < 2);
            if (gate) {
                _Pragma("unroll 1") for (unsigned n = 0; n < SPIN; ++n) {
                    CFENCE();
#pragma unroll
                    for (int i = 0; i < 4; ++i) if (!okm[i]) s[i] = LD4(rw, (lane >> 2) * 128 + (lane & 3) * 16 + i * 2048, O_ST + j * 8192);
#pragma unroll
                    for (int i = 0; i < 4; ++i) okm[i] = (s[i].y == tag) & (s[i].w == tag);
                    if (__all(okm[0] & okm[1] & okm[2] & okm[3])) break;
                    __builtin_amdgcn_s_sleep(3);
                }
#pragma unroll
                for (int i = 0; i < 4; ++i) okm[i] = false;
            }
            _Pragma("unroll 1") for (unsigned n = 0; n < SPIN; ++n) {
                CFENCE();
#pragma unroll
                for (int i = 0; i < 4; ++i) if (!okm[i]) { if (!gate) s[i] = LD4(rw, (lane >> 2) * 128 + (lane & 3) * 16 + i * 2048, O_ST + j * 8192); xg[i] = LD4(rw, lane * 16 + i * 1024, O_XG + b * 16384 + j * 4096); }
                if (!okd) md = LD4(rw, mo, O_MD);
#pragma unroll
                for (int i = 0; i < 4; ++i) okm[i] = (s[i].y == tag) & (s[i].w == tag) & (xg[i].y == tag) & (xg[i].w == tag);
                okd = ((md.y == tag) & (md.w == tag)) | (lane >= 8);
                if (__all(okm[0] & okm[1] & okm[2] & okm[3] & okd)) break;
                __builtin_amdgcn_s_sleep(3);
            }
            CFENCE();
            STAMP(4);
            v2u xt[4];
#pragma unroll
            for (int i = 0; i < 4; ++i) xt[i] = (v2u){xg[i].x, xg[i].z};
            const bool one = ((lane >> 2) == 1) | ((lane >> 2) == 4);
            auto modv = [&]() { float m0 = asf(md.x), m1 = asf(md.z); m0 = ad1(m0, dppf<0xB1>(m0)); m1 = ad1(m1, dppf<0xB1>(m1));
                unsigned mp = pkb(m0, m1); if (one) mp = pkb(ad1(1.f, blo(mp)), ad1(1.f, bhi(mp))); return mp; };
            unsigned mp = modv();
            const unsigned psa = __builtin_amdgcn_readlane(mp, 0), pca = __builtin_amdgcn_readlane(mp, 4);
            SETM0(61440 + j * 1024);
            const f2 sa = {blo(psa), bhi(psa)}, ca = {blo(pca), bhi(pca)};
#pragma unroll
            for (int i = 0; i < 4; ++i) {
                f2 x0 = {blo(xt[i].x), bhi(xt[i].x)}, x1 = {blo(xt[i].y), bhi(xt[i].y)};
                float r0 = __builtin_amdgcn_rcpf(bhi(s[i].x)), r1 = __builtin_amdgcn_rcpf(bhi(s[i].z));
                f2 h0 = __builtin_elementwise_fma((x0 - blo(s[i].x)) * r0, ca, sa), h1 = __builtin_elementwise_fma((x1 - blo(s[i].z)) * r1, ca, sa);
                unsigned pa = pkb(h0.x, h1.x), pb = pkb(h0.y, h1.y);
                WTID(pa, L_H + 512 - 61440 + i * 256); WTID(pb, L_H + H_CH + 512 - 61440 + i * 256);
                L2(L_EX + j * 8192 + i * 512 + lane * 8) = xt[i];
            }
            PAD(1);
            sig(lds, F_H);
            __builtin_amdgcn_s_setprio(0);
            STAMP(5);
            if constexpr (!EARLY) {
#pragma unroll
                for (int i = 0; i < 7; ++i) W1P(i);
            }
            if (j == 0) {
                _Pragma("unroll 1") for (unsigned n = 0; n < SPIN; ++n) {
                    if (__all((md.y == tag) & (md.w == tag))) break;
                    CFENCE();
                    md = LD4(rw, mo, O_MD);
                }
                mp = modv();
                const unsigned pga = __builtin_amdgcn_readlane(mp, 8), pcb = __builtin_amdgcn_readlane(mp, 16);
                if (lane == 0) L4(L_MV) = (v4u){pga, pcb, tag, 0};
                sig(lds, F_EX);
            }
        }
        v2u b1v, b1w;
        {
            v4u mc[5];
            const unsigned go = (lane & 1) * 24576 + (2560 + cb2 * 64 + (lane >> 1) * 2) * 8;
#pragma unroll
            for (int i = 0; i < 4; ++i) mc[i] = LD4(rw, lane * 16, O_MD + (i & 1) * 24576 + (3 + (i >> 1)) * 4096 + j * 1024);
            mc[4] = LD4(rw, go, O_MD);
            b1v = *(const v2u*)(a.b1 + mem1 * 112 + j * 16 + q * 4);
            b1w = *(const v2u*)(a.b1 + mem1 * 112 + (j < 3 ? j + 4 : j) * 16 + q * 4);
            SB();
            wtl(lds, F_AF, 4);
            wtl(lds, F_H, 4);
            if (WMODE == 0) {
#pragma unroll
                for (int i = 7; i < 28; ++i) W1P(i);
            } else {
                w2run(4, (8 + j) * 7168); w2run(4, (12 + j) * 7168);
#pragma unroll
                for (int i = 7; i < 14; ++i) W1P(i);
                VM(7);
#pragma unroll
                for (int i = 0; i < 8; i += 2) W2R(i, (8 + (i >> 2) * 4 + j) * 7168 + (i & 3) * 1024);
                LG0();
#pragma unroll
                for (int i = 14; i < 28; ++i) W1P(i);
            }
            if (b == 0 && j == 0 && lane == 0) ST1(tag, rw, 0, O_E1);
            _Pragma("unroll 1") for (unsigned n = 0; n < SPIN; ++n) {
                bool ok = true;
#pragma unroll
                for (int i = 0; i < 5; ++i) ok &= (mc[i].y == tag) & (mc[i].w == tag);
                if (__all(ok)) break;
                CFENCE();
#pragma unroll
                for (int i = 0; i < 4; ++i) mc[i] = LD4(rw, lane * 16, O_MD + (i & 1) * 24576 + (3 + (i >> 1)) * 4096 + j * 1024);
                mc[4] = LD4(rw, go, O_MD);
            }
            const unsigned psb = pkb(ad1(asf(mc[0].x), asf(mc[1].x)), ad1(asf(mc[0].z), asf(mc[1].z)));
            const unsigned pcb = pkb(ad1(asf(mc[2].x), asf(mc[3].x)), ad1(asf(mc[2].z), asf(mc[3].z)));
            float g0 = asf(mc[4].x), g1 = asf(mc[4].z);
            g0 = ad1(g0, dppf<0xB1>(g0)); g1 = ad1(g1, dppf<0xB1>(g1));
            const unsigned cso = (((j - rot) & 3) * 64 + (lane & 48) + ((lane >> 1) & 3) * 4 + ((lane >> 3) & 1) * 2 + (lane & 1)) * 4;
            L1(L_CS + cso) = pkb(ad1(1.f, blo(pcb)), ad1(1.f, bhi(pcb))); L1(L_CS + CS_B + cso) = psb;
            L1(L_GB + (lane >> 1) * 4) = pkb(g0, g1);
            VM(0);
            sig(lds, F_W1);
            STAMP(6);
        }
        {
            wtl(lds, F_W1, EARLY ? 8 : 4);
            f4 d0 = {0, 0, 0, 0}, d1 = {0, 0, 0, 0};
            unsigned wa = (4 * q + (r16 >> 2)) * 224 + (r16 & 3) * 8 + j * 32, wb = wa + (j < 3 ? 128 : 0);
            unsigned ca = L_CS + (r16 & 1) * CS_B + q * 16;
#pragma unroll 1
            for (int g = 0; g < 2; ++g) {
                v4u bs[8], a0[8], a1[8];
#pragma unroll
                for (int u = 0; u < 8; ++u) {
                    bs[u] = L4(ca + u * 64);
                    a0[u] = trd2(lds, wa + u * 7168, wa + u * 7168 + 3584); a1[u] = trd2(lds, wb + u * 7168, wb + u * 7168 + 3584);
                    SB();
                    if (u >= 2) { d0 = mm16(a0[u - 2], bs[u - 2], d0); d1 = mm16(a1[u - 2], bs[u - 2], d1); }
                    SB();
                }
#pragma unroll
                for (int u = 6; u < 8; ++u) { d0 = mm16(a0[u], bs[u], d0); d1 = mm16(a1[u], bs[u], d1); }
                wa += 57344; wb += 57344; ca += 512;
            }
            if (r16 == 1) {
                d0 += (f4){blo(b1v.x), bhi(b1v.x), blo(b1v.y), bhi(b1v.y)};
                d1 += (f4){blo(b1w.x), bhi(b1w.x), blo(b1w.y), bhi(b1w.y)};
            }
            if (r16 < 2) {
#pragma unroll
                for (int e = 0; e < 4; ++e) {
                    float h = rbf(d0[e]); L1(L_AP + (j * 16 + q * 4 + e) * 8 + r16 * 4) = pkb(h, d0[e] - h);
                    if (j < 3) { float g = rbf(d1[e]); L1(L_AP + ((j + 4) * 16 + q * 4 + e) * 8 + r16 * 4) = pkb(g, d1[e] - g); }
                }
            }
            if (lane < 2) L1(L_ZR + lane * 4) = 0;
            STAMP(14);
        }
        SB();
        if (WMODE == 1) { wtl(lds, F_ZD, 4); w2run(10, L_H + j * 10240); }
        wtl(lds, F_E2, 4);
        __builtin_amdgcn_s_setprio(GP);
        STAMP(7);
        {
            v4u sx1[2];
#pragma unroll
            for (int i = 0; i < 2; ++i) sx1[i] = LD4(rw, (lane >> 3) * 512 + (lane & 7) * 16, O_X1 + team1 * 131072 + ((mem1 >> 2) * 64 + j * 16 + i * 8) * 512 + (mem1 & 3) * 128);
            float sm[4], sq[4];
#pragma unroll
            for (int e = 0; e < 4; ++e) {
                unsigned u0 = sx1[0][e], u1 = sx1[1][e];
                float v0 = blo(u0), v1 = bhi(u0), v2 = blo(u1), v3 = bhi(u1);
                sm[e] = ad1(ad1(v0, v1), ad1(v2, v3));
                sq[e] = fm1(v0, v0, fm1(v1, v1, fm1(v2, v2, mu1(v3, v3))));
                asm volatile("s_nop 1" : "+v"(sm[e]), "+v"(sq[e]));
            }
            float sA = sw32add(sm[0], sm[2]), sB = sw32add(sm[1], sm[3]), qA = sw32add(sq[0], sq[2]), qB = sw32add(sq[1], sq[3]);
            asm volatile("s_nop 1" : "+v"(sA), "+v"(sB), "+v"(qA), "+v"(qB));
            float s1 = sw16add(sA, sB), q1 = sw16add(qA, qB);
            asm volatile("s_nop 1" : "+v"(s1), "+v"(q1));
            s1 = ad1(s1, dppf<0x128>(s1)); q1 = ad1(q1, dppf<0x128>(q1));
            if (!(lane & 8)) LF2(L_PJ + j * 256 + (q * 8 + (lane & 7)) * 8) = (f2){s1, q1};
            if (sigr(lds, F_PJ) == 3) {
                const unsigned pa = L_PJ + (lane & 31) * 8;
                const f2 t0 = LF2(pa), t1 = LF2(pa + 256), t2 = LF2(pa + 512), t3 = LF2(pa + 768);
                if (lane < 32) { v4u g = {asu(ad1(ad1(t0.x, t1.x), ad1(t2.x, t3.x))), tag, asu(ad1(ad1(t0.y, t1.y), ad1(t2.y, t3.y))), tag}; ST4(g, rw, lane * 16, O_PS + team1 * 8192 + (mem1 & 3) * 2048 + (mem1 >> 2) * 512); }
            }
            SB();
            STAMP(8);
            if (WMODE == 1) {
#pragma unroll
                for (int i = 0; i < 10; i += 2) W2R(8 + i, L_H + j * 10240 + i * 1024);
            }
            if (WMODE == 2 || (WMODE == 0 && RINGE)) w2run(10, L_H + j * 10240); else __builtin_amdgcn_s_sleep(PSD);
            v4u p8[2];
            _Pragma("unroll 1") for (unsigned n = 0; n < SPIN; ++n) {
                CFENCE();
#pragma unroll
                for (int i = 0; i < 2; ++i) p8[i] = LD4(rw, lane * 16 + i * 1024, O_PS + team1 * 8192 + j * 2048);
                if (__all((p8[0].y == tag) & (p8[0].w == tag) & (p8[1].y == tag) & (p8[1].w == tag))) break;
                __builtin_amdgcn_s_sleep(4);
            }
            CFENCE();
            float ss = ad1(asf(p8[0].x), asf(p8[1].x)), sx = ad1(asf(p8[0].z), asf(p8[1].z));
            asm volatile("s_nop 1" : "+v"(ss), "+v"(sx));
            ss = sw32add(ss, ss); sx = sw32add(sx, sx);
            const float mean = mu1(ss, 1.f / 512.f);
            const float var = rbf(__builtin_fmaxf(__builtin_fmaf(-mean, mean, mu1(sx, 1.f / 512.f)), 0.f));
            const float den = rbf(__builtin_amdgcn_sqrtf(rbf(var + 1e-5f)));
            const int e = (lane >> 3) & 3, u = (lane & 7) + 8 * (e >> 1) + 16 * (e & 1);
            if (lane < 32) { L2(L_BT + (j * 32 + u) * 8) = (v2u){pkb(-mean, -mean), pkb(den, den)}; LF1(L_R + (j * 32 + u) * 4) = __builtin_amdgcn_rcpf(den); }
            sig(lds, F_ST);
            __builtin_amdgcn_s_setprio(0);
            STAMP(9);
            if (WMODE == 0) {
                if (!RINGE) w2run(10, L_H + j * 10240);
                VM(0);
#pragma unroll
                for (int i = 0; i < 10; i += 2) W2R(i, L_H + j * 10240 + i * 1024);
                LG0();
                w2run(NR - 10, L_H + j * 10240);
                VM(0);
#pragma unroll
                for (int i = 10; i < NR; i += 2) W2R(i, L_H + j * 10240 + (i - 10) * 1024);
            }
            if (WMODE == 2) {
                VM(0);
#pragma unroll
                for (int i = 0; i < 10; i += 2) W2R(8 + i, L_H + j * 10240 + i * 1024);
            }
            sig(lds, F_RD);
            STAMP(3);
        }
        {
            const int dt = j >> 1, tq = j & 1, u = lane & 31, hh = lane >> 5;
            unsigned xr[8];
            {
                const unsigned xo = (2 * hh) * 512 + (u & 7) * 16 + (2 * ((u >> 3) & 1) + (u >> 4)) * 4;
#pragma unroll
                for (int i = 0; i < 8; ++i) xr[i] = LD1(rw, xo + ((i >> 1) & 1) * 2048 + (i & 1) * 512, O_X1 + team2 * 131072 + (cb2 * 32 + dt * 16) * 512 + (2 * th + tq) * 128 + (i >> 2) * 4096);
            }
            v2u b2v[4];
#pragma unroll
            for (int i = 0; i < 4; ++i) b2v[i] = *(const v2u*)(a.b2 + cb2 * 64 + dt * 32 + i * 8 + hh * 4);
            SB();
            wtl(lds, F_RD, 8);
            w2run(56 - NR, j * W2_W);
            STAMP(10);
            _Pragma("unroll 1") for (unsigned n = 0; n < SPIN; ++n) {
                CFENCE();
                unsigned f = LD1(rw, ((lane & 3) * 4 + ((lane >> 2) & 1)) * 128, O_F3 + (team2 * 64 + j * 16 + th * 2) * 128);
                if (__all(f == tag)) break;
            }
            CFENCE();
            PAD(3);
            STAMP(11);
            f16v c2[4];
#pragma unroll
            for (int i = 0; i < 4; ++i)
#pragma unroll
                for (int e = 0; e < 16; ++e) c2[i][e] = 0.f;
            {
                const unsigned hb = O_H + team2 * 458752 + th * 229376 + j * 28672;
                constexpr int HW = HWIN;
                v4u h[HW][2];
                v4u wl[2][2];
                const unsigned hv0 = lane * 16, hv1 = lane * 16 + 114688, ra0 = j * W2_W + w2ra, ra1 = j * W2_W + (w2ra ^ 64);
                unsigned ho = hb + rk * 1024; const unsigned he = hb + 28672;
#define HL1(g, i) do { h[(g) % HW][i] = LD4(rw, (i) ? hv1 : hv0, ho); if (i) { ho += 1024; if (ho == he) ho = hb; asm volatile("" : "+s"(ho)); } } while (0)
#pragma unroll
                for (int g = 0; g < HW - 1; ++g) { HL1(g, 0); SB(); HL1(g, 1); SB(); }
#pragma unroll
                for (int g = 0; g < 28; ++g) {
                    const int gn = g + 1;
                    v4u a0, a1;
                    if (2 * g < NR) { a0 = wf[2 * g]; a1 = wf[2 * g + 1]; } else { a0 = wl[g & 1][0]; a1 = wl[g & 1][1]; }
                    const unsigned src = (2 * gn - NR) * 1024;
                    c2[0] = mm32(a0, h[g % HW][0], c2[0]);
                    if (g + HW - 1 < 28) HL1(g + HW - 1, 0);
                    SB();
                    c2[2] = mm32(a1, h[g % HW][0], c2[2]);
                    if (2 * gn >= NR && gn < 28) wl[gn & 1][0] = trd2(lds, src + ra0, src + ra0 + 512);
                    SB();
                    c2[1] = mm32(a0, h[g % HW][1], c2[1]);
                    if (g + HW - 1 < 28) HL1(g + HW - 1, 1);
                    SB();
                    c2[3] = mm32(a1, h[g % HW][1], c2[3]);
                    if (2 * gn >= NR && gn < 28) wl[gn & 1][1] = trd2(lds, src + ra1, src + ra1 + 512);
                    SB();
                }
            }
            STAMP(12);
            f16v own;
#pragma unroll
            for (int i = 0; i < 4; ++i) {
                if (i == j) own = c2[i];
                else {
#pragma unroll
                    for (int e = 0; e < 4; ++e) LF4(j * W2_W + i * 4096 + e * 1024 + lane * 16) = (f4){c2[i][4 * e], c2[i][4 * e + 1], c2[i][4 * e + 2], c2[i][4 * e + 3]};
                }
            }
            sig(lds, F_RD);
            wt(lds, F_RD, 12);
            v2u o[4];
#pragma unroll
            for (int e = 0; e < 4; ++e) {
                f4 v = {own[4 * e], own[4 * e + 1], own[4 * e + 2], own[4 * e + 3]};
                f4 p1 = LF4(((j + 1) & 3) * W2_W + j * 4096 + e * 1024 + lane * 16), p2 = LF4(((j + 2) & 3) * W2_W + j * 4096 + e * 1024 + lane * 16), p3 = LF4(((j + 3) & 3) * W2_W + j * 4096 + e * 1024 + lane * 16);
                v = (v + p1) + (p2 + p3);
                v2u g = L2(L_GB + (dt * 16 + e * 4 + hh * 2) * 4);
                v += (f4){blo(b2v[e].x), bhi(b2v[e].x), blo(b2v[e].y), bhi(b2v[e].y)};
                o[e].x = pkb(__builtin_fmaf(blo(g.x), v[0], blo(xr[2 * e])), __builtin_fmaf(bhi(g.x), v[1], bhi(xr[2 * e])));
                o[e].y = pkb(__builtin_fmaf(blo(g.y), v[2], blo(xr[2 * e + 1])), __builtin_fmaf(bhi(g.y), v[3], bhi(xr[2 * e + 1])));
            }
            const int tok = team2 * 128 + 16 * (u & 7) + 4 * (2 * th + tq) + 2 * ((u >> 3) & 1) + (u >> 4);
            u16* op = a.out + tok * 512 + cb2 * 64 + dt * 32 + hh * 8;
#pragma unroll
            for (int e = 0; e < 4; e += 2) {
                v2u rx_ = __builtin_amdgcn_permlane32_swap(o[e].x, o[e + 1].x, false, false);
                v2u ry_ = __builtin_amdgcn_permlane32_swap(o[e].y, o[e + 1].y, false, false);
                *(v4u*)(op + e * 8) = (v4u){rx_.x, ry_.x, rx_.y, ry_.y};
            }
        }
        asm volatile(".Lkend:");
        STAMP(13);
        STAMP_OUT(16);
        return;
    }

    const int ch = j >> 1, hf = j & 1;
    unsigned par[8];
    {
        const unsigned po = ((2 * b + ch) * 128 + hf * 64 + lane) * 2;
        asm volatile("global_load_ushort %0, %7, %8" NTS "\n\tglobal_load_ushort %1, %7, %9" NTS "\n\tglobal_load_ushort %2, %7, %10" NTS "\n\tglobal_load_ushort %3, %7, %11" NTS "\n\t"
                     "global_load_ushort %4, %7, %12" NTS "\n\tglobal_load_ushort %5, %7, %13" NTS "\n\tglobal_load_ushort %6, %14, %15"
                     : "=&v"(par[0]), "=&v"(par[1]), "=&v"(par[2]), "=&v"(par[3]), "=&v"(par[4]), "=&v"(par[5]), "=&v"(par[6])
                     : "v"(po), "s"(a.Are), "s"(a.Aim), "s"(a.Bre), "s"(a.Bim), "s"(a.Cre), "s"(a.Cim), "v"((2 * b + ch) * 2), "s"(a.ldt) : "memory");
        asm volatile("global_load_ushort %0, %1, %2" : "=&v"(par[7]) : "v"((2 * b + ch) * 2), "s"(a.Dsk) : "memory");
        const v4u z4 = {0, 0, 0, 0};
        if (threadIdx.x < 64) L4(L_H + (threadIdx.x >> 5) * H_CH + (threadIdx.x & 31) * 16) = z4;
        if (lane == 0) L2(ch ? L_T1 : L_T0) = (v2u){0, 0};
        if (threadIdx.x >= 224) L1(L_FLG + (threadIdx.x & 31) * 4) = 0;
        LG0();
    }
    __builtin_amdgcn_s_barrier();
    unsigned tag;
    {
        unsigned ta[19], wa_[12], vo1[7];

        const rsrc_t rw1 = mk(a.w1, 512 * 1792 * 2);
        if constexpr (EARLY) { VO1(); }
        if constexpr (EARLY == 2) {
#pragma unroll
            for (int i = 0; i < 7; ++i) W1P(i);
        }
        {
            const unsigned tb_ = (ch ? L_T1 : L_T0) + hf * 4;
#pragma unroll
            for (int t = 0; t < 3; ++t)
#pragma unroll
                for (int e = 0; e < 4; ++e) {
                    int v;
                    if (t == 0) v = 512 * q + 128 * e;
                    else if (t == 1) v = q < 2 ? 512 * q + 128 * e + 4 : 256 * (q - 2) + 128 * (e >> 1) + 2 + 4 * (e & 1);
                    else v = q < 2 ? 128 * q + 1 + 2 * e : 2048;
                    v += 8 * r16;
                    int idx = v < 256 ? v + 1 : (int)(asu((float)v) >> 16) - (0x4300 - 129);
                    if (t == 2 && q >= 2 && !(e == 0 && lane == 32)) idx = 642;
                    wa_[t * 4 + e] = tb_ + (idx + (idx >> 4)) * 8; asm volatile("" : "+v"(wa_[t * 4 + e]));
                }
        }
        {
            const int u = hf * 64 + lane;
            const unsigned tb_ = ch ? L_T1 : L_T0;
            const bool small = u >= 113;
#pragma unroll
            for (int e = 0; e < 20; e += 2) {
                const int l0 = 2047 - 16 * u - e;
                const unsigned p = pkb((float)l0, (float)(l0 - 1));
                int i0 = small ? l0 + 1 : (int)(p & 0xffff) - (0x4300 - 129), i1 = small ? l0 : (int)(p >> 16) - (0x4300 - 129);
                if (e >= 16) { i0 = max(i0, 0); i1 = max(i1, 0); }
                ta[e] = tb_ + (i0 + (i0 >> 4)) * 8; asm volatile("" : "+v"(ta[e]));
                if (e + 1 < 19) { ta[e + 1] = tb_ + (i1 + (i1 >> 4)) * 8; asm volatile("" : "+v"(ta[e + 1])); }
            }
        }
        asm volatile("s_waitcnt vmcnt(%8)" : "+v"(par[0]), "+v"(par[1]), "+v"(par[2]), "+v"(par[3]), "+v"(par[4]), "+v"(par[5]), "+v"(par[6]), "+v"(par[7]) : "i"(EARLY == 2 ? 7 : 0) : "memory");
        const float dsk = (hf == 1 && lane == 63) ? blo(par[7]) : 0.f;
        STAMP(1);
        const float dt = rbf(__builtin_amdgcn_exp2f(blo(par[6]) * 1.44269504f));
        const float lr2 = dt * blo(par[0]) * 1.44269504f, rev = dt * blo(par[1]) * 0.15915494f;
        const float br = dt * blo(par[2]), bi = dt * blo(par[3]), cr = blo(par[4]), ci = blo(par[5]);
        f2 W = {cr * br - ci * bi, cr * bi + ci * br};
        unsigned mx = max(asu(W.x) & 0x7fffffffu, asu(W.y) & 0x7fffffffu);
        mx = max(mx, asu(dppf<0x128>(asf(mx)))); mx = max(mx, asu(dppf<0x124>(asf(mx)))); mx = max(mx, asu(dppf<0x122>(asf(mx)))); mx = max(mx, asu(dppf<0x121>(asf(mx))));
        const unsigned mu = max(max((unsigned)__builtin_amdgcn_readlane(mx, 0), (unsigned)__builtin_amdgcn_readlane(mx, 16)), max((unsigned)__builtin_amdgcn_readlane(mx, 32), (unsigned)__builtin_amdgcn_readlane(mx, 48)));
        const unsigned ex = min(max(mu >> 23, 30u), 230u);
        const float sc = asf((266u - ex) << 23), isc = asf((ex - 12u) << 23);
        SETM0(j * XY_W);
#define PUT(t, row, v) WTID(pkh(v), L_XY + (t) * XY_T + (row) * 272)
        {
            const f2 y8 = cexpn(lr2, -rev, 8.f), y8p = perp(y8);
            const f2 z1 = cexpn(lr2, rev, 1.f), z1p = perp(z1), z2 = cm(z1, z1, z1p), z2p = perp(z2), z4 = cm(z2, z2, z2p), z4p = perp(z4);
            const f2 zc = cexpn(lr2, rev, 128.f), zcp = perp(zc);
            f2 X = W * sc, y = {1.f, 0.f};
#pragma unroll
            for (int i = 0; i < 16; ++i) {
                const f2 Xn = cm(X, zc, zcp), yn = i == 0 ? y8 : cm(y, y8, y8p);
                PUT(1, i, X); PUT(0, i, y);
                if (i < 8) {
                    f2 X4 = cm(X, z4, z4p); PUT(2, i, X4);
                    if (i < 4) {
                        f2 X2 = cm(X, z2, z2p), X6 = cm(X4, z2, z2p); PUT(2, 8 + 2 * i, X2); PUT(2, 9 + 2 * i, X6);
                        if (i < 2) { PUT(3, 4 * i, cm(X, z1, z1p)); PUT(3, 4 * i + 1, cm(X2, z1, z1p)); PUT(3, 4 * i + 2, cm(X4, z1, z1p)); PUT(3, 4 * i + 3, cm(X6, z1, z1p)); }
                    }
                }
                X = Xn; y = yn;
            }
            PUT(3, 8, X);
        }
        STAMP(14);
        SB();
        {
            const unsigned fa = L_XY + j * XY_W + r16 * 272 + q * 16;
            v4u fr[4][4];
#pragma unroll
            for (int s = 0; s < 4; ++s)
#pragma unroll
                for (int t = 0; t < 4; ++t) fr[s][t] = L4(fa + t * XY_T + s * 64);
            SB();
            f4 d[3] = {{0, 0, 0, 0}, {0, 0, 0, 0}, {0, 0, 0, 0}};
#pragma unroll
            for (int s = 0; s < 4; ++s)
#pragma unroll
                for (int t = 0; t < 3; ++t) d[t] = mm16h(fr[s][t + 1], fr[s][0], d[t]);
#pragma unroll
            for (int t = 0; t < 3; ++t)
#pragma unroll
                for (int e = 0; e < 4; ++e) LF1(wa_[t * 4 + e]) = d[t][e] * isc;
        }
        sig(lds, F_KP0 + ch);
        if constexpr (EARLY == 1) {
#pragma unroll
            for (int i = 0; i < 7; ++i) W1P(i);
        }
        wt(lds, F_KP0 + ch, 2);
        STAMP(2);
        const unsigned kib = L_XY + ch * KI_CH;
        {
            f2 kp[19]; float kv[19];
#pragma unroll
            for (int e = 0; e < 19; ++e) kp[e] = LF2(ta[e]);
            SB();
#pragma unroll
            for (int e = 0; e < 19; ++e) kv[e] = kp[e].x + kp[e].y;
            kv[15] += dsk;
            const unsigned ka = kib + (hf * 64 + lane) * 32;
#pragma unroll
            for (int c = 0; c < 4; ++c)
#pragma unroll
                for (int hx = 0; hx < 2; ++hx) {
                    const int e = c + 8 * hx;
                    v4u pk_;
#pragma unroll
                    for (int i = 0; i < 4; ++i) { unsigned t_ = pkb(kv[e + 2 * i], kv[e + 2 * i + 1]); asm volatile("" : "+v"(t_)); pk_[i] = t_; }
                    L4(ka + c * KI_C + hx * 16) = pk_;
                }
            if (lane < 4) L4(kib + lane * KI_C + 4096 + hf * 16) = (v4u){0, 0, 0, 0};
        }
        sig(lds, F_KE0 + ch); wt(lds, F_KE0 + ch, 2);
        STAMP(15);
        v4u af[4][8];
        {
            const unsigned aa = kib + ((3 - r16) & 3) * KI_C + 2 * (2028 - 4 * (r16 >> 2) + 8 * q - 128 * hf - 32 * 59);
#pragma unroll
            for (int d = 0; d < 4; ++d)
#pragma unroll
                for (int t = 0; t < 8; ++t) { const unsigned o = aa + 64 * (59 - d - 8 * t);
                    v2u p0 = L2V(o), p1 = L2V(o + 8); af[d][t] = (v4u){p0.x, p0.y, p1.x, p1.y}; }
        }
        sig(lds, F_AF);
        STAMP(3);
        wt(lds, F_H, 4);
        STAMP(4);
        __builtin_amdgcn_s_setprio(1);
        f4 xs[2][2];
        f4 acc[8];
#pragma unroll
        for (int c = 0; c < 8; ++c) acc[c] = (f4){0, 0, 0, 0};
        unsigned ea = L_EX + j * 8192 + lane * 16;
        {
            const unsigned ha = L_H + ch * H_CH + 2 * (240 + 16 * r16 + 8 * q - 128 * hf - 96);
            unsigned ba = L_EX + j * 8192 + (16 * r16 + 4 * q) * 4;
            asm volatile("" : "+v"(ba), "+v"(ea));
            v4u bf[2][8], xr[2];
#pragma unroll
            for (int e = 0; e < 8; ++e) { bf[0][e] = L4(ha + 64 * (8 * e + 3)); SB(); }
#pragma unroll
            for (int d = 0; d < 3; ++d) {
                int n = 0;
#pragma unroll
                for (int e = 0; e < 8; ++e)
#pragma unroll
                    for (int t = 0; t + e < 8; ++t) {
                        acc[t + e] = mm16(af[d][t], bf[d & 1][e], acc[t + e]);
                        SB();
                        if (n % 3 == 1 && n < 24) bf[(d + 1) & 1][n / 3] = L4(ha + 64 * (8 * (n / 3) + 3 - (d + 1)));
                        if (d == 0 && (n == 24 || n == 25)) xr[n & 1] = L4(ba + (n & 1) * 1024);
                        if (n == 33) __builtin_amdgcn_s_waitcnt(0xc07f);
                        if (d == 1 && n >= 24 && n < 32) { const int k = n - 24; unsigned u_ = xr[k >> 2][k & 3]; float lo = blo(u_), hi = bhi(u_); asm volatile("" : "+v"(lo), "+v"(hi)); xs[0][k >> 2][k & 3] = lo; xs[1][k >> 2][k & 3] = hi; }
                        ++n;
                        SB();
                    }
            }
            if constexpr (EARLY) { VM(0); sig(lds, F_W1); }
#pragma unroll
            for (int c = 7; c >= 0; --c) {
#pragma unroll
                for (int t = 0; t <= c; ++t) {
                    acc[c] = mm16(af[3][t], bf[1][c - t], acc[c]);
                    SB();
                    if (c < 7 && t == (c < 2 ? c : 2)) LF4(ea + (c + 1) * 1024) = acc[c + 1];
                    SB();
                }
            }
            LF4(ea) = acc[0];
        }
        STAMP(5);
        sig(lds, F_EX);
        unsigned so = ((lane >> 3) & 1) * 131072 + q * 128 + (lane & 7) * 16 + O_Z + 4 * j * 131072 + b * 512;
        unsigned pa = L_EX + 2 * j * 1024 + lane * 16;
        asm volatile("" : "+v"(so), "+v"(pa));
        wt(lds, F_EX, 5);
        {
            const v4u mv = L4(L_MV);
            f4 p[2][4];
#pragma unroll
            for (int t = 0; t < 2; ++t)
#pragma unroll
                for (int i = 0; i < 4; ++i) p[t][i] = LF4(pa + i * 8192 + t * 1024);
            SB();
            tag = mv.z;
            const float ga0 = blo(mv.x), ga1 = bhi(mv.x), c10 = blo(mv.y), c11 = bhi(mv.y);
#pragma unroll
            for (int t = 0; t < 2; ++t) {
                f4 u0 = __builtin_elementwise_fma(p[t][0] + p[t][1], (f4){ga0, ga0, ga0, ga0}, xs[0][t]), u1 = __builtin_elementwise_fma(p[t][2] + p[t][3], (f4){ga1, ga1, ga1, ga1}, xs[1][t]);
                asm volatile("" : "+v"(u0), "+v"(u1));
                f4 s0 = u0 * c10, s1 = u1 * c11;
                asm volatile("" : "+v"(s0), "+v"(s1));
                v4u xo, zo;
#pragma unroll
                for (int e = 0; e < 4; ++e) { xo[e] = pkb(u0[e], u1[e]); zo[e] = pkb(s0[e], s1[e]); }
                ST4(zo, rw, so, 2 * t * 131072);
                ST4(xo, rw, so, O_X1 - O_Z + 2 * t * 131072);
            }
        }
        __builtin_amdgcn_s_setprio(0);
        STAMP(6);
        VM(0);
        if (sigr(lds, F_ZD) == 3) { if (lane == 0) ST1(tag, rw, 0, O_F2 + b * 128); }
        STAMP(7);
    }
    {
        {
            bool ok = false;
            if (P2A) __builtin_amdgcn_s_sleep(P2A);
            _Pragma("unroll 1") for (unsigned n = 0; n < SPIN; ++n) {
                CFENCE();
                if (!ok) ok = LD1(rw, lane * 128, O_F2 + j * 8192) == tag;
                if (__all(ok)) break;
                __builtin_amdgcn_s_sleep(P2B);
            }
            CFENCE();
            PAD(2);
            sig(lds, F_E2);
            wt(lds, F_E2, 4);
        }
        STAMP(8);
        f4 c1[14];
#pragma unroll
        for (int i = 0; i < 14; ++i) c1[i] = (f4){0, 0, 0, 0};
        {
            const unsigned zo = (lane >> 3) * 512 + (lane & 7) * 16, zo2 = zo + 4096, zb = O_Z + team1 * 131072 + j * 128;
            unsigned wa = (4 * q + (r16 >> 2)) * 224 + (r16 & 3) * 8;
            v4u z[4][2];
            v2u wv[2][14];
#define ZL1(sl, i, so) z[sl][i] = LD4(rw, (i) ? zo2 : zo, so)
#define XCH(d0, d1, m, a0, b0, a1, b1) asm volatile("s_mov_b64 vcc, %2\n\tv_cndmask_b32_dpp %0, %3, %4, vcc row_ror:8 row_mask:0xf bank_mask:0xf\n\tv_cndmask_b32_dpp %1, %5, %6, vcc row_ror:8 row_mask:0xf bank_mask:0xf" \
                : "=&v"(d0), "=&v"(d1) : "s"(m), "v"(a0), "v"(b0), "v"(a1), "v"(b1) : "vcc")
            const unsigned long long mlo = 0x00ff00ff00ff00ffull, mhi = ~mlo;
#define XPART(k, zz, A, B) do { unsigned x_, y_; if ((k) & 1) XCH(x_, y_, mhi, zz.x, zz.z, zz.y, zz.w); else XCH(x_, y_, mlo, zz.z, zz.x, zz.w, zz.y); A[k] = x_; B[k] = y_; } while (0)
            unsigned g0 = zb + (rot & 3) * 32768;
#pragma unroll
            for (int u = 0; u < 4; ++u)
#pragma unroll
                for (int i = 0; i < 2; ++i) { ZL1(u, i, g0 + u * 8192); SB(); }
            g0 = zb + ((rot + 1) & 3) * 32768;
            wt(lds, F_W1, EARLY ? 8 : 4);
            __builtin_amdgcn_s_setprio(1);
#pragma unroll
            for (int t = 0; t < 14; ++t) wv[0][t] = trd(lds, wa + (t >> 1) * 32 + (t & 1) * 3584);
            v4u fa, fb;
#pragma unroll
            for (int k = 0; k < 4; ++k) XPART(k, z[0][k >> 1], fa, fb);
            SB(); ZL1(0, 0, g0); SB(); ZL1(0, 1, g0); SB();
            __builtin_amdgcn_s_waitcnt(0xc07f);
            STAMP(9);
            auto body = [&](auto last_, unsigned gc, unsigned gn) {
                constexpr bool last = decltype(last_)::value;
#pragma unroll
                for (int u = 0; u < 4; ++u) {
                    v4u na, nb;
                    const int s1 = (u + 1) & 3;
#pragma unroll
                    for (int t = 0; t < 14; ++t) {
                        const v2u w0 = wv[u & 1][t & ~1], w1_ = wv[u & 1][t | 1];
                        c1[t] = mm16((v4u){w0.x, w0.y, w1_.x, w1_.y}, (t & 1) ? fb : fa, c1[t]);
                        SB();
                        if (!(last && u == 3)) {
                            if (t < 7) {
                                wv[(u + 1) & 1][2 * t] = trd(lds, wa + (u + 1) * 7168 + t * 32);
                                wv[(u + 1) & 1][2 * t + 1] = trd(lds, wa + (u + 1) * 7168 + t * 32 + 3584);
                            }
                            if (t >= 7 && t < 11) XPART(t - 7, z[s1][(t - 7) >> 1], na, nb);
                            if (t == 12) __builtin_amdgcn_s_waitcnt(0xc07f);
                        }
                        if ((t == 11 || t == 13) && !last) ZL1(s1, (t - 11) >> 1, u < 3 ? gc + (u + 1) * 8192 : gn);
                        if (last && u == 3 && t == 6) sig(lds, F_RD);
                        SB();
                    }
                    fa = na; fb = nb;
                }
            };
#pragma unroll 1
            for (int it = 0; it < 3; ++it) {
                const unsigned g1 = zb + ((it + 2 + rot) & 3) * 32768;
                body(std::false_type{}, g0, g1);
                g0 = g1; wa += 28672;
            }
            body(std::true_type{}, g0, g0);
            asm volatile("" :: "v"(z[0][0]), "v"(z[0][1]));
        }
        STAMP(10);
        wt(lds, F_ST, 4);
        {
            const v2u bA = L2(q ? L_ZR : L_BT + (j * 32 + r16) * 8), bB = L2(q ? L_ZR : L_BT + (j * 32 + 16 + r16) * 8);
            const float rA = LF1(L_R + (j * 32 + r16) * 4), rB = LF1(L_R + (j * 32 + 16 + r16) * 4);
            const unsigned hb = O_H + team1 * 458752 + j * 114688 + mem1 * 7168;
#pragma unroll
            for (int t = 0; t < 7; ++t) {
                const v2u ap = L2(L_AP + (t * 16 + r16) * 8);
                f4 uA = mm16s(ap, bA, c1[2 * t]) * rA, uB = mm16s(ap, bB, c1[2 * t + 1]) * rB;
#pragma unroll
                for (int e = 0; e < 4; ++e) { uA[e] = __builtin_fmaxf(uA[e], 0.f); uB[e] = __builtin_fmaxf(uB[e], 0.f); }
                uA *= uA; uB *= uB;
                v2u s0 = __builtin_amdgcn_permlane16_swap(pkb(uA[0], uA[1]), pkb(uB[0], uB[1]), false, false);
                v2u s1 = __builtin_amdgcn_permlane16_swap(pkb(uA[2], uA[3]), pkb(uB[2], uB[3]), false, false);
                ST4(((v4u){s0.x, s1.x, s0.y, s1.y}), rw, lane * 16, hb + t * 1024);
            }
        }
        STAMP(11);
        VM(0);
        if (lane == 0) ST1(tag, rw, 0, O_F3 + ((team1 * 16 + mem1) * 4 + j) * 128);
        STAMP(12);
    }
    STAMP_OUT(0);
}

extern "C" void kernel_launch(void* const* d_in, const int* in_sizes, int n_in, void* d_out, int out_size, void* d_ws, size_t ws_size, hipStream_t stream) {
    Args a;
    a.x = (const u16*)d_in[0]; a.Are = (const u16*)d_in[1]; a.Aim = (const u16*)d_in[2]; a.Bre = (const u16*)d_in[3]; a.Bim = (const u16*)d_in[4];
    a.Cre = (const u16*)d_in[5]; a.Cim = (const u16*)d_in[6]; a.ldt = (const u16*)d_in[7]; a.Dsk = (const u16*)d_in[8];
    a.w1 = (const u16*)d_in[10]; a.b1 = (const u16*)d_in[11]; a.w2 = (const u16*)d_in[12]; a.b2 = (const u16*)d_in[13];
    a.c = (const u16*)d_in[14]; a.wm = (const u16*)d_in[15];
    a.out = (u16*)d_out; a.ws = (char*)d_ws + (WSEND ? ((ws_size - WSB) & ~(size_t)0x1fffff) : 0); a.st = (unsigned long long*)((char*)d_ws + (32u << 20));
    (void)hipFuncSetAttribute((const void*)fused, hipFuncAttributeMaxDynamicSharedMemorySize, LDS_BYTES);
    fused<<<256, 512, LDS_BYTES, stream>>>(a);
}
```

```cpp
#include <hip/hip_runtime.h>
#include <stdint.h>
#include <type_traits>
#include <utility>

typedef unsigned short u16;
typedef unsigned v4u __attribute__((ext_vector_type(4)));
typedef unsigned v2u __attribute__((ext_vector_type(2)));
typedef float f2 __attribute__((ext_vector_type(2)));
typedef float f4 __attribute__((ext_vector_type(4)));
typedef float f16v __attribute__((ext_vector_type(16)));
typedef short s4 __attribute__((ext_vector_type(4)));
typedef __bf16 bf2 __attribute__((ext_vector_type(2)));
typedef __bf16 bf8 __attribute__((ext_vector_type(8)));
typedef _Float16 h2 __attribute__((ext_vector_type(2)));
typedef _Float16 h8 __attribute__((ext_vector_type(8)));
typedef __amdgpu_buffer_rsrc_t rsrc_t;
#define LAS __attribute__((address_space(3)))
#define DEV __device__ __forceinline__

constexpr int PDLY = 10, OAUX = 0, P2L = 10, P2A = 8;
constexpr int HWIN = 5, SK = 5, ZPRE = 8, ZPER = 3, PSK = 12;

struct Args {
    const u16 *x, *Are, *Aim, *Bre, *Bim, *Cre, *Cim, *ldt, *Dsk, *w1, *b1, *w2, *b2, *c, *wm;
    u16* out; char* ws;
};

constexpr unsigned O_E1 = 0, O_ST = 4096, O_MD = 65536, O_F2 = 393216, O_F3 = 262144, O_PS = 524288;
constexpr unsigned O_Z = 4u << 20, O_X1 = 6u << 20, O_H = 8u << 20, O_XG = 16u << 20, WSB = 20u << 20;
constexpr unsigned L_XY = 28672, XY_W = 15504, XY_T = 4352, KI_C = 4160, KI_CH = 2 * XY_W, L_MS = L_XY + 4 * XY_W, L_SI = L_MS + 16384, L_T0 = L_SI + 512;
constexpr unsigned L_H = 114688, H_CH = 4608, L_EX = 123904, L_XS = L_EX + 2048, L_T1 = L_EX + 16384 + 2048;
constexpr unsigned L_MV = 156672, L_CS = L_MV + 128, CS_B = 1152, L_GB = L_CS + 2304, L_AP = L_GB + 128, L_ZR = L_AP + 896, L_BT = L_AP + 1024, L_R = L_BT + 1024, L_SP = L_R + 512, L_JK = L_SP + 64, L_PJ = L_JK;
constexpr unsigned L_FLG = 163840 - 128, LDS_BYTES = 163840;
static_assert(L_JK + 1024 <= L_FLG, "lds");
constexpr int NR = 18;
enum { F_XTD = 0, F_KP0, F_KP1, F_KE0, F_KE1, F_AF, F_H, F_EX, F_ZD, F_E2, F_W1, F_ST, F_RD, F_MJ, F_PJ, F_HD, F_SL0 };
constexpr unsigned SPIN = 1u << 17;

template <class F, int... I> DEV void sfor_(F&& f, std::integer_sequence<int, I...>) { (f(std::integral_constant<int, I>{}), ...); }
template <int N, class F> DEV void sfor(F&& f) { sfor_(f, std::make_integer_sequence<int, N>{}); }
DEV float asf(unsigned v) { return __uint_as_float(v); }
DEV unsigned asu(float v) { return __float_as_uint(v); }
DEV float blo(unsigned v) { return asf(v << 16); }
DEV float bhi(unsigned v) { return asf(v & 0xffff0000u); }
DEV unsigned pkb(float a, float b) { bf2 r = __builtin_convertvector((f2){a, b}, bf2); return __builtin_bit_cast(unsigned, r); }
DEV unsigned pkh(f2 a) { h2 r = __builtin_convertvector(a, h2); return __builtin_bit_cast(unsigned, r); }
DEV float rbf(float a) { return blo(pkb(a, a)); }
DEV float ad1(float a, float b) { float r; asm("v_add_f32 %0, %1, %2" : "=v"(r) : "v"(a), "v"(b)); return r; }
DEV float mu1(float a, float b) { float r; asm("v_mul_f32 %0, %1, %2" : "=v"(r) : "v"(a), "v"(b)); return r; }
DEV float fm1(float a, float b, float c) { float r; asm("v_fma_f32 %0, %1, %2, %3" : "=v"(r) : "v"(a), "v"(b), "v"(c)); return r; }
DEV unsigned rfl(unsigned v) { return __builtin_amdgcn_readfirstlane(v); }
template <int C> DEV float dppf(float v) { return asf((unsigned)__builtin_amdgcn_update_dpp(0, (int)asu(v), C, 0xf, 0xf, true)); }
DEV float red16(float v) { v += dppf<0x128>(v); v += dppf<0x124>(v); v += dppf<0x122>(v); v += dppf<0x121>(v); return v; }
DEV float sw32add(float a, float b) { v2u r = __builtin_amdgcn_permlane32_swap(asu(a), asu(b), false, false); unsigned x = r.x, y = r.y; return ad1(asf(x), asf(y)); }
DEV float sw16add(float a, float b) { v2u r = __builtin_amdgcn_permlane16_swap(asu(a), asu(b), false, false); unsigned x = r.x, y = r.y; return ad1(asf(x), asf(y)); }
DEV rsrc_t mk(const void* p, unsigned n) { return __builtin_amdgcn_make_buffer_rsrc((void*)p, 0, n, 0x00020000); }
DEV void dma(rsrc_t r, unsigned voff, unsigned soff, unsigned ldst) {
    asm volatile("s_mov_b32 m0, %0\n\ts_nop 0\n\tbuffer_load_dwordx4 %1, %2, %3 offen lds" :: "s"(ldst), "v"(voff), "s"(r), "s"(soff) : "memory");
}
#define SETM0(v) asm volatile("s_mov_b32 m0, %0\n\ts_nop 0" :: "s"(v) : "memory")
#define WTID(v, off) asm volatile("ds_write_addtid_b32 %0 offset:%1" :: "v"(v), "i"(off) : "memory")
#define NE2(a, b) __builtin_amdgcn_bitop3_b32(a, b, tag, 0x7e)
#define SB() __builtin_amdgcn_sched_barrier(0)
#define VM(n) asm volatile("s_waitcnt vmcnt(%0)" :: "i"(n) : "memory")
#define LG0() asm volatile("s_waitcnt lgkmcnt(0)" ::: "memory")
#define CFENCE() asm volatile("" ::: "memory")
#define LD4(r, vo, so) __builtin_amdgcn_raw_buffer_load_b128(r, vo, so, 16)
#define LD2(r, vo, so) __builtin_amdgcn_raw_buffer_load_b64(r, vo, so, 16)
#define LD1(r, vo, so) __builtin_amdgcn_raw_buffer_load_b32(r, vo, so, 16)
#define ST4(v, r, vo, so) __builtin_amdgcn_raw_buffer_store_b128(v, r, (vo) + (so), 0, 16)
#define ST1(v, r, vo, so) __builtin_amdgcn_raw_buffer_store_b32(v, r, vo, so, 16)
#define L4(a) (*(LAS v4u*)(lds + (a)))
#define L2(a) (*(LAS v2u*)(lds + (a)))
#define L2V(a) (*(volatile LAS v2u*)(lds + (a)))
#define L1(a) (*(LAS unsigned*)(lds + (a)))
#define LF4(a) (*(LAS f4*)(lds + (a)))
#define LF2(a) (*(LAS f2*)(lds + (a)))
#define LF1(a) (*(LAS float*)(lds + (a)))
typedef LAS char* lptr;
DEV v2u trd(lptr lds, unsigned a) { s4 v = __builtin_amdgcn_ds_read_tr16_b64_v4i16((LAS s4*)(lds + a)); return __builtin_bit_cast(v2u, v); }
DEV v4u trd2(lptr lds, unsigned a, unsigned b) { v2u p = trd(lds, a), q = trd(lds, b); return (v4u){p.x, p.y, q.x, q.y}; }
DEV f4 mm16(v4u a, v4u b, f4 c) { return __builtin_amdgcn_mfma_f32_16x16x32_bf16(__builtin_bit_cast(bf8, a), __builtin_bit_cast(bf8, b), c, 0, 0, 0); }
#define MMA(c, a, b) asm volatile("v_mfma_f32_16x16x32_bf16 %0, %1, %2, %0" : "+v"(c) : "v"(a), "v"(b))
DEV f4 mm16h(v4u a, v4u b, f4 c) { return __builtin_amdgcn_mfma_f32_16x16x32_f16(__builtin_bit_cast(h8, a), __builtin_bit_cast(h8, b), c, 0, 0, 0); }
DEV f4 mm16s(v2u a, v2u b, f4 c) { return __builtin_amdgcn_mfma_f32_16x16x16bf16_1k(__builtin_bit_cast(s4, a), __builtin_bit_cast(s4, b), c, 0, 0, 0); }
DEV f16v mm32(v4u a, v4u b, f16v c) { return __builtin_amdgcn_mfma_f32_32x32x16_bf16(__builtin_bit_cast(bf8, a), __builtin_bit_cast(bf8, b), c, 0, 0, 0); }

DEV void sign(lptr lds, int i) {
    const unsigned ad = (unsigned)(size_t)lds + L_FLG + i * 4, one = 1;
    asm volatile("s_mov_b64 exec, 1\n\tds_add_u32 %0, %1\n\ts_mov_b64 exec, -1" :: "v"(ad), "v"(one) : "memory");
}
DEV unsigned sigr(lptr lds, int i) {
    const unsigned ad = (unsigned)(size_t)lds + L_FLG + i * 4, one = 1; unsigned o;
    asm volatile("s_mov_b64 exec, 1\n\tds_add_rtn_u32 %0, %1, %2\n\ts_mov_b64 exec, -1\n\ts_waitcnt lgkmcnt(0)" : "=&v"(o) : "v"(ad), "v"(one) : "memory");
    return rfl(o);
}
template <int SLEEP> DEV void wts(lptr lds, int i, unsigned n) {
    _Pragma("unroll 1") for (unsigned s = 0; s < (SPIN << 6); ++s) {
        unsigned v = __hip_atomic_load((LAS unsigned*)(lds + L_FLG) + i, __ATOMIC_RELAXED, __HIP_MEMORY_SCOPE_WORKGROUP);
        if (rfl(v) >= n) break;
        if (SLEEP) __builtin_amdgcn_s_sleep(SLEEP);
    }
    CFENCE();
}
#define wt wts<0>
#define wtl wts<1>
DEV f2 cm(f2 a, f2 m, f2 mp) { f2 t = m * (f2){a.x, a.x}; return __builtin_elementwise_fma(mp, (f2){a.y, a.y}, t); }
DEV f2 perp(f2 m) { return (f2){-m.y, m.x}; }
DEV f2 cexpn(float lr2, float rev, float p) {
    float mg = __builtin_amdgcn_exp2f(lr2 * p), ph = __builtin_amdgcn_fractf(rev * p);
    return (f2){mg * __builtin_amdgcn_cosf(ph), mg * __builtin_amdgcn_sinf(ph)};
}


struct LagTab { unsigned short v[128][24]; };
constexpr int bf16_round(int l) { int s = 0; while ((l >> s) >= 256) ++s; if (!s) return l; int h = 1 << (s - 1), r = l & ((1 << s) - 1), t = l >> s; if (r > h || (r == h && (t & 1))) ++t; return t << s; }
constexpr LagTab make_tab() {
    LagTab T{};
    for (int u = 0; u < 128; ++u)
        for (int e = 0; e < 24; ++e) {
            const int l = 2047 - 16 * u - e;
            int row = 47, col = 15;
            if (l >= 0) {
                const int v = bf16_round(l), lo = v & 7, hi = v >> 7;
                col = (v >> 3) & 15;
                row = v == 2048 ? 40 : (lo & 1) ? 32 + 4 * hi + (lo >> 1) : (lo & 2) ? 24 + 2 * hi + (lo >> 2) : (lo << 2) + hi;
            }
            T.v[u][e] = (unsigned short)((row >> 4) * 1024 + (((row >> 2) & 3) * 16 + col) * 16 + (row & 3) * 4);
        }
    return T;
}
__device__ const __attribute__((aligned(16))) LagTab lag_tab = make_tab();
#define W1P(i) dma(rw1, vo1[(i) % 7], ((((i) / 7 + rot) & 3) * 4 + j) * (32 * 3584), (((i) / 7 * 4 + j) * 7 + (i) % 7) * 1024)
#define VO1() _Pragma("unroll") for (int i = 0; i < 7; ++i) { int c = i * 64 + lane; vo1[i] = (c / 14) * (3584 - 224) + c * 16 + mem1 * 224; }

__global__ void __launch_bounds__(512, 2) fused(Args a) {
    extern __shared__ __attribute__((aligned(16))) char smem[];
    lptr lds = (lptr)smem;
    const int w = rfl(threadIdx.x >> 6), lane = threadIdx.x & 63, j = w & 3;
    const int xcd = blockIdx.x & 7, ix = blockIdx.x >> 3, b = xcd * 32 + ix;
    const int team1 = (xcd >> 1) * 4 + (ix >> 3), mem1 = (xcd & 1) * 8 + (ix & 7);
    const int team2 = team1, th = (ix >> 2) & 1, cb2 = (xcd & 1) * 4 + (ix & 3), rot = ix >> 3, rk = ((ix >> 2) * 7) >> 1;
    const int q = lane >> 4, r16 = lane & 15;
    {
        unsigned long long pc; unsigned len;
        asm volatile(".Lkbeg:\n\ts_getpc_b64 %0\n\ts_mov_b32 %1, .Lkend-.Lkbeg" : "=s"(pc), "=s"(len));
        dma(mk((const void*)pc, len), lane * 16, (w * 32 + ix) * 1024, L_JK);
    }
    L1(L_FLG + (threadIdx.x & 31) * 4) = 0;
    __builtin_amdgcn_sched_barrier(0x4);
    const rsrc_t rw = mk(a.ws, WSB);
    const int ch = j >> 1, hf = j & 1;
    unsigned par[8];
    v2u b1v, lt2;
    v4u lt0, lt1;
    const bool domod = ix < 24;
    const int kq = (ix & 1) * 2 + (j & 1), mcb = ((ix >> 2) * 8 + xcd) * 2 + ((ix >> 1) & 1);
    unsigned ep, cv; v4u xa[4];
    if (w >= 4) {
        __builtin_amdgcn_s_setprio(2);
        const rsrc_t rx = mk(a.x, 2048 * 512 * 2);
        asm volatile("buffer_load_dword %0, off, %1, 0 sc1" : "=v"(ep) : "s"(rw) : "memory");
        if (j < 2) {
#pragma unroll
            for (int i = 0; i < 4; ++i) dma(rx, q * 1024 + ((r16 ^ (2 * i + (q >> 1))) * 16), ((b >> 1) * 16 + 4 * i) * 1024 + (b & 1) * 512 + j * 256, L_XS + j * 8192 + i * 1024);
            asm volatile("global_load_dword %0, %1, %2" : "=v"(cv) : "v"(kq * 256 + lane * 4), "s"(a.c) : "memory");
            const rsrc_t rm = mk(a.wm, domod ? 512 * 3072 * 2 : 0);
#pragma unroll 1
            for (int i = 0; i < 8; ++i) dma(rm, (lane >> 2) * 6144 + (lane & 3) * 16, (kq * 128 + i * 16) * 6144 + mcb * 64, L_MS + j * 8192 + i * 1024);
        } else {
#pragma unroll
            for (int i = 0; i < 4; ++i) asm volatile("buffer_load_dwordx4 %0, %1, %2, %3 offen offset:%4" : "=v"(xa[i]) : "v"(q * 1024 + r16 * 16), "s"(rx), "s"((b * 8 + (j & 1) * 4) * 1024), "i"(i * 256) : "memory");
        }
    } else {
        const unsigned po = ((2 * b + ch) * 128 + hf * 64 + lane) * 2;
        asm volatile("global_load_ushort %0, %7, %8\n\tglobal_load_ushort %1, %7, %9\n\tglobal_load_ushort %2, %7, %10\n\tglobal_load_ushort %3, %7, %11\n\t"
                     "global_load_ushort %4, %7, %12\n\tglobal_load_ushort %5, %7, %13\n\tglobal_load_ushort %6, %14, %15"
                     : "=&v"(par[0]), "=&v"(par[1]), "=&v"(par[2]), "=&v"(par[3]), "=&v"(par[4]), "=&v"(par[5]), "=&v"(par[6])
                     : "v"(po), "s"(a.Are), "s"(a.Aim), "s"(a.Bre), "s"(a.Bim), "s"(a.Cre), "s"(a.Cim), "v"((2 * b + ch) * 2), "s"(a.ldt) : "memory");
        asm volatile("global_load_ushort %0, %1, %2" : "=&v"(par[7]) : "v"((2 * b + ch) * 2), "s"(a.Dsk) : "memory");
        asm volatile("global_load_dwordx2 %0, %1, %2" : "=&v"(b1v) : "v"((mem1 * 112 + j * 16 + q * 4) * 2), "s"(a.b1) : "memory");
        asm volatile("global_load_dwordx4 %0, %3, %4\n\tglobal_load_dwordx4 %1, %3, %4 offset:16\n\tglobal_load_dwordx2 %2, %3, %4 offset:32" : "=&v"(lt0), "=&v"(lt1), "=&v"(lt2)
                     : "v"((hf * 64 + lane) * 48), "s"(&lag_tab) : "memory");
    }
    LG0();
    __builtin_amdgcn_s_barrier();
    int wrole = w;
    asm volatile("" : "+s"(wrole));
    auto st_job = [&](v2u bv, int tile) __attribute__((always_inline)) {
        bv &= (r16 & 1) ? ~0u : 0u;
        f4 d0 = {blo(bv.x), bhi(bv.x), blo(bv.y), bhi(bv.y)};
        unsigned wa = (4 * q + (r16 >> 2)) * 224 + (r16 & 3) * 8 + tile * 32, wb = wa + 57344, ca = L_CS + (r16 & 1) * CS_B + q * 16, ao = L_AP + r16 * 4 + tile * 128 + q * 32;
        asm volatile("" : "+v"(d0), "+v"(wa), "+v"(wb), "+v"(ca), "+v"(ao));
        wt(lds, F_W1, 4);
        v4u bs[16], a0[16];
#pragma unroll
        for (int u = 0; u < 16; ++u) {
            bs[u] = L4(ca + u * 64);
            a0[u] = trd2(lds, (u < 8 ? wa : wb) + (u & 7) * 7168, (u < 8 ? wa : wb) + (u & 7) * 7168 + 3584);
            SB();
            if (u >= 4) d0 = mm16(a0[u - 4], bs[u - 4], d0);
            SB();
        }
#pragma unroll
        for (int u = 12; u < 16; ++u) d0 = mm16(a0[u], bs[u], d0);
        v4u o0;
#pragma unroll
        for (int e = 0; e < 4; ++e) { const float v0 = d0[e], h0 = rbf(v0); float l0 = v0 - h0; asm volatile("" : "+v"(l0)); o0[e] = pkb(h0, l0); }
        if (r16 < 2) { L1(ao) = o0.x; L1(ao + 8) = o0.y; L1(ao + 16) = o0.z; L1(ao + 24) = o0.w; }
    };
    auto ps_job = [&](v4u x0, v4u x1, unsigned tag) __attribute__((always_inline)) {
        const unsigned a_pj = L_PJ + j * 256 + (q * 8 + (lane & 7)) * 8, a_pr = L_PJ + ((lane & 7) + ((lane & 8) << 1) + ((lane & 16) >> 1)) * 8;
        const unsigned a_ps = lane * 16 + O_PS + team1 * 8192 + (mem1 & 3) * 2048 + (mem1 >> 2) * 512;
        float sm[4], sq[4];
#pragma unroll
        for (int e = 0; e < 4; ++e) {
            const unsigned u0 = x0[e], u1 = x1[e];
            const float v0 = blo(u0), v1 = bhi(u0), v2 = blo(u1), v3 = bhi(u1);
            sm[e] = ad1(ad1(v0, v1), ad1(v2, v3));
            sq[e] = fm1(v0, v0, fm1(v1, v1, fm1(v2, v2, mu1(v3, v3))));
        }
        asm volatile("s_nop 1" : "+v"(sm[0]), "+v"(sq[0]), "+v"(sm[1]), "+v"(sq[1]), "+v"(sm[2]), "+v"(sq[2]), "+v"(sm[3]), "+v"(sq[3]));
        float sA = sw32add(sm[0], sm[2]), sB = sw32add(sm[1], sm[3]), qA = sw32add(sq[0], sq[2]), qB = sw32add(sq[1], sq[3]);
        asm volatile("s_nop 1" : "+v"(sA), "+v"(sB), "+v"(qA), "+v"(qB));
        float s1 = sw16add(sA, sB), q1 = sw16add(qA, qB);
        asm volatile("s_nop 1" : "+v"(s1), "+v"(q1));
        s1 = ad1(s1, dppf<0x128>(s1)); q1 = ad1(q1, dppf<0x128>(q1));
        LF2(a_pj) = (f2){s1, q1};
        if (sigr(lds, F_PJ) == 3) {
            const f2 t0 = LF2(a_pr), t1 = LF2(a_pr + 256), t2 = LF2(a_pr + 512), t3 = LF2(a_pr + 768);
            const v4u g = {asu(ad1(ad1(t0.x, t1.x), ad1(t2.x, t3.x))), tag, asu(ad1(ad1(t0.y, t1.y), ad1(t2.y, t3.y))), tag};
            asm volatile("s_mov_b32 exec_hi, 0\n\tbuffer_store_dwordx4 %0, %1, %2, 0 offen sc1\n\ts_mov_b32 exec_hi, -1\n\ts_nop 0" :: "v"(g), "v"(a_ps), "s"(rw) : "memory");
        }
        SB();
    };
    const unsigned x1b = O_X1 + team1 * 131072 + ((mem1 >> 2) * 64 + j * 16) * 512 + (mem1 & 3) * 128;
    if (wrole >= 4) {
        unsigned tag;
        const rsrc_t rw1 = mk(a.w1, 512 * 1792 * 2), rw2 = mk(a.w2, 512 * 1792 * 2);
        unsigned vo1[7];
        v4u wf[NR];
        const unsigned w2vo = j * 458752 + (lane >> 3) * 1024 + cb2 * 128 + (((lane & 7) ^ (((lane >> 4) & 1) << 2)) * 16);
        const unsigned w2v1 = w2vo + 8192;
        auto w2run = [&](int p0, int n, unsigned dst, int thr = -1) __attribute__((always_inline)) {
            unsigned o = rk * 16384 + p0 * 8192; if (o >= 458752) o -= 458752;
            asm volatile("s_mov_b32 m0, %0" :: "s"(dst - 1024) : "memory");
#pragma unroll
            for (int i = 0; i < n; i += 2) {
                if (thr == 0) VM(0); else if (thr == 2) VM(2); else if (thr == 4) VM(4);
                asm volatile("s_addk_i32 m0, 0x400\n\ts_add_u32 %1, %0, 0x4000\n\tbuffer_load_dwordx4 %2, %4, %0 offen lds\n\ts_addk_i32 m0, 0x400\n\ts_cmp_lg_u32 %1, 0x70000\n\t"
                             "buffer_load_dwordx4 %3, %4, %0 offen lds\n\ts_cselect_b32 %0, %1, 0"
                             : "+s"(o), "=&s"(dst) : "v"(w2vo), "v"(w2v1), "s"(rw2) : "memory", "scc");
            }
        };
#define W2R(i, src) do { wf[i] = trd2(lds, (src) + w2ra, (src) + w2ra + 512); wf[(i) + 1] = trd2(lds, (src) + (w2ra ^ 64), (src) + (w2ra ^ 64) + 512); } while (0)
        if (wrole < 6) {
            asm volatile("s_waitcnt vmcnt(8)\n\tv_add_u32 %0, 1, %2" : "=v"(tag), "+v"(cv) : "v"(ep) : "memory");
            v4u t4[4];
#pragma unroll
            for (int i = 0; i < 4; ++i) t4[i] = L4(L_XS + j * 8192 + (2 * (lane & 7) + (i & 1)) * 256 + ((((lane >> 3) + 8 * (i >> 1)) ^ (lane & 7)) * 16));
#pragma unroll
            for (int i = 0; i < 8; ++i) { v4u g = {t4[(i >> 2) * 2][i & 3], tag, t4[(i >> 2) * 2 + 1][i & 3], tag};
                ST4(g, rw, ((lane >> 3) * 4 + (i & 3) + (i >> 2) * 32) * 16384 + (lane & 7) * 16, O_XG + ((b & 1) * 128 + j * 64) * 16384 + (b >> 1) * 128); }
            SB();
            const float c0 = blo(cv), c1 = bhi(cv);
            const unsigned sv = pkb(c0 * __builtin_amdgcn_rcpf(1.f + __builtin_amdgcn_exp2f(-1.44269504f * c0)), c1 * __builtin_amdgcn_rcpf(1.f + __builtin_amdgcn_exp2f(-1.44269504f * c1)));
            L1(L_SI + j * 256 + lane * 4) = sv;
            v4u bs[4];
#pragma unroll
            for (int s = 0; s < 4; ++s) bs[s] = L4(L_SI + j * 256 + s * 64 + q * 16);
            const unsigned ma = L_MS + j * 8192 + (8 * q + (r16 >> 2)) * 64 + (r16 & 3) * 8;
            VM(8);
            v4u am[8];
#pragma unroll
            for (int s = 0; s < 8; ++s) am[s] = trd2(lds, ma + (s >> 1) * 2048 + (s & 1) * 32, ma + (s >> 1) * 2048 + (s & 1) * 32 + 256);
            SB();
            f4 d0 = {0, 0, 0, 0}, d1 = {0, 0, 0, 0};
#pragma unroll
            for (int s = 0; s < 4; ++s) { d0 = mm16(am[2 * s], bs[s], d0); d1 = mm16(am[2 * s + 1], bs[s], d1); }
            LF4(L_MS + j * 8192 + (r16 & 1) * 64 + q * 16) = (r16 & 1) ? d1 : d0;
            if (sigr(lds, F_MJ) == 1) {
                const f2 e = LF2(L_MS + r16 * 8) + LF2(L_MS + 8192 + r16 * 8);
                if (domod && lane < 16) { v4u g = {asu(e.x), tag, asu(e.y), tag}; ST4(g, rw, lane * 16, O_MD + (ix & 1) * 24576 + mcb * 256); }
            }
        } else {
            asm volatile("s_waitcnt vmcnt(0)\n\tv_add_u32 %0, 1, %5" : "=v"(tag), "+v"(xa[0]), "+v"(xa[1]), "+v"(xa[2]), "+v"(xa[3]) : "v"(ep) : "memory");
            f2 v[16]; f2 s2 = {0.f, 0.f};
#pragma unroll
            for (int i = 0; i < 4; ++i)
#pragma unroll
                for (int e = 0; e < 4; ++e) { unsigned u = xa[i][e]; v[i * 4 + e] = (f2){blo(u), bhi(u)}; s2 += v[i * 4 + e]; }
            const float mean = red16(s2.x + s2.y) * (1.f / 512.f);
            f2 q2 = {0.f, 0.f};
#pragma unroll
            for (int i = 0; i < 16; ++i) { f2 dd = v[i] - mean; q2 = __builtin_elementwise_fma(dd, dd, q2); }
            const float var = rbf(red16(q2.x + q2.y) * (1.f / 512.f));
            const float den = __builtin_amdgcn_sqrtf(rbf(var + 1e-5f));
            if (r16 == 0) L1(L_SP + ((j & 1) * 4 + q) * 4) = pkb(mean, den);
            if (sigr(lds, F_XTD) == 1) {
                v2u p = L2(L_SP + (lane & 3) * 8);
                if (lane < 8) { v4u g = {p.x, tag, p.y, tag}; ST4(g, rw, lane * 16, O_ST + b * 128); }
            }
        }
        __builtin_amdgcn_s_setprio(0);
        SB();
        const unsigned w2ra = ((q >> 1) * 1024 + (r16 >> 2) * 128 + (((2 * (q & 1) + ((r16 & 3) >> 1)) ^ ((r16 >> 3) << 2)) * 16) + 8 * (r16 & 1)) ^ ((j >> 1) << 6);
        VO1();
        {
            v4u s[4], xg[4], md;
            const unsigned mo = (lane & 1) * 24576 + (min(lane >> 2, 4) * 512 + 2 * b) * 8;
#pragma unroll
            for (int i = 0; i < 7; ++i) W1P(i);
            if (j >= 2) wtl(lds, F_MJ, 2);
            __builtin_amdgcn_s_sleep(PDLY);
            float one = ((0x000f00f0u >> (lane & 31)) & 1) ? 1.f : 0.f;
            unsigned mk8 = lane < 8 ? ~0u : 0u, xa_ = L_EX + j * 8192 + lane * 8;
            asm volatile("" : "+v"(one), "+v"(mk8), "+v"(xa_));
            const unsigned so_ = (lane >> 2) * 128 + (lane & 3) * 16;
#define E1LD() do { _Pragma("unroll") for (int i = 0; i < 4; ++i) { xg[i] = LD4(rw, lane * 16 + i * 1024, O_XG + b * 16384 + j * 4096); s[i] = LD4(rw, so_ + (i & 1) * 2048, O_ST + j * 8192 + (i >> 1) * 4096); } md = LD4(rw, mo, O_MD); SB(); } while (0)
            E1LD();
            _Pragma("unroll 1") for (unsigned n = 0; n < SPIN; ++n) {
                unsigned bad = NE2(md.y, md.w) & mk8;
#pragma unroll
                for (int i = 0; i < 4; ++i) bad |= NE2(s[i].y, s[i].w) | NE2(xg[i].y, xg[i].w);
                if (__all(bad == 0)) break;
                __builtin_amdgcn_s_sleep(2);
                CFENCE();
                if (bad) E1LD();
            }
            CFENCE();
            __builtin_amdgcn_s_setprio(2);
            v2u xt[4];
#pragma unroll
            for (int i = 0; i < 4; ++i) xt[i] = (v2u){xg[i].x, xg[i].z};
            auto modv = [&]() { float m0 = asf(md.x), m1 = asf(md.z); m0 = ad1(m0, dppf<0xB1>(m0)); m1 = ad1(m1, dppf<0xB1>(m1));
                const unsigned mp = pkb(m0, m1); return pkb(ad1(one, blo(mp)), ad1(one, bhi(mp))); };
            unsigned mp = modv();
            const unsigned psa = __builtin_amdgcn_readlane(mp, 0), pca = __builtin_amdgcn_readlane(mp, 4);
            SETM0(61440 + j * 1024);
            const f2 sa = {blo(psa), bhi(psa)}, ca = {blo(pca), bhi(pca)};
#pragma unroll
            for (int i = 0; i < 4; ++i) {
                f2 x0 = {blo(xt[i].x), bhi(xt[i].x)}, x1 = {blo(xt[i].y), bhi(xt[i].y)};
                float r0 = __builtin_amdgcn_rcpf(bhi(s[i].x)), r1 = __builtin_amdgcn_rcpf(bhi(s[i].z));
                f2 h0 = __builtin_elementwise_fma((x0 - blo(s[i].x)) * r0, ca, sa), h1 = __builtin_elementwise_fma((x1 - blo(s[i].z)) * r1, ca, sa);
                unsigned pa = pkb(h0.x, h1.x), pb = pkb(h0.y, h1.y);
                WTID(pa, L_H + 512 - 61440 + i * 256); WTID(pb, L_H + H_CH + 512 - 61440 + i * 256);
                L2(xa_ + i * 512) = xt[i];
            }
            sign(lds, F_H);
            __builtin_amdgcn_s_setprio(0);
            if (j == 0) {
                _Pragma("unroll 1") for (unsigned n = 0; n < SPIN; ++n) {
                    if (__all(NE2(md.y, md.w) == 0)) break;
                    CFENCE();
                    md = LD4(rw, mo, O_MD);
                }
                mp = modv();
                const unsigned pga = __builtin_amdgcn_readlane(mp, 8), pcb = __builtin_amdgcn_readlane(mp, 16);
                if (lane == 0) L4(L_MV) = (v4u){pga, pcb, tag, 0};
                sign(lds, F_EX);
            }
        }
        v2u g1w;
        v4u raw[8];
#pragma unroll
        for (int i = 0; i < 8; ++i) { raw[i] = __builtin_amdgcn_raw_buffer_load_b128(rw2, w2vo, rk * 16384 + i * 8192, 0); SB(); }
        {
            v4u mc[5];
            const unsigned go = (lane & 1) * 24576 + (2560 + cb2 * 64 + (lane >> 1) * 2) * 8;
#pragma unroll
            for (int i = 0; i < 4; ++i) mc[i] = LD4(rw, lane * 16, O_MD + (i & 1) * 24576 + (3 + (i >> 1)) * 4096 + j * 1024);
            mc[4] = LD4(rw, go, O_MD);
            g1w = *(const v2u*)(a.b1 + mem1 * 112 + (j < 3 ? j + 4 : j) * 16 + q * 4);
            SB();
            wtl(lds, F_H, 4);
            wtl(lds, F_KE0, 2); wtl(lds, F_KE1, 2);
#pragma unroll
            for (int i = 21; i < 28; ++i) W1P(i);
            wtl(lds, F_AF, 4);
#pragma unroll
            for (int i = 7; i < 21; ++i) W1P(i);
            _Pragma("unroll 1") for (unsigned n = 0; n < SPIN; ++n) {
                bool ok = true;
#pragma unroll
                for (int i = 0; i < 5; ++i) ok &= (mc[i].y == tag) & (mc[i].w == tag);
                if (__all(ok)) break;
                CFENCE();
#pragma unroll
                for (int i = 0; i < 4; ++i) mc[i] = LD4(rw, lane * 16, O_MD + (i & 1) * 24576 + (3 + (i >> 1)) * 4096 + j * 1024);
                mc[4] = LD4(rw, go, O_MD);
            }
            const unsigned psb = pkb(ad1(asf(mc[0].x), asf(mc[1].x)), ad1(asf(mc[0].z), asf(mc[1].z)));
            const unsigned pcb = pkb(ad1(asf(mc[2].x), asf(mc[3].x)), ad1(asf(mc[2].z), asf(mc[3].z)));
            float g0 = asf(mc[4].x), g1 = asf(mc[4].z);
            g0 = ad1(g0, dppf<0xB1>(g0)); g1 = ad1(g1, dppf<0xB1>(g1));
            const unsigned cso = (((j - rot) & 3) * 64 + (lane & 48) + ((lane >> 1) & 3) * 4 + ((lane >> 3) & 1) * 2 + (lane & 1)) * 4;
            L1(L_CS + cso) = pkb(ad1(1.f, blo(pcb)), ad1(1.f, bhi(pcb))); L1(L_CS + CS_B + cso) = psb;
            L1(L_GB + (lane >> 1) * 4) = pkb(g0, g1);
            VM(0);
            sign(lds, F_W1);
        }
        SB();
        const unsigned ring = L_H + j * 10240;
        wtl(lds, F_ZD, 4);
#pragma unroll
        for (int i = 0; i < 8; ++i) L4(ring + i * 1024 + lane * 16) = raw[i];
#pragma unroll
        for (int i = 0; i < 8; i += 2) W2R(i, ring + i * 1024);
        LG0();
        w2run(8, 10, ring, 4);
        if (j < 3) st_job(g1w, j + 4);
        sign(lds, F_ST);
        VM(0);
#pragma unroll
        for (int i = 0; i < 10; i += 2) W2R(8 + i, ring + i * 1024);
        LG0();
        w2run(18, 10, ring, 4);
        wtl(lds, F_E2, 4);
        {
            const unsigned xo_ = (lane >> 3) * 512 + (lane & 7) * 16;
            const v4u x0 = LD4(rw, xo_, x1b), x1 = LD4(rw, xo_, x1b + 4096);
            __builtin_amdgcn_s_setprio(2);
            ps_job(x0, x1, tag);
            __builtin_amdgcn_s_setprio(0);
        }
        if (b == 0 && j == 0 && lane == 0) ST1(tag, rw, 0, O_E1);
        {
            const int dt = j >> 1, tq = j & 1, u = lane & 31, hh = lane >> 5;
            wtl(lds, F_ST, 8);
            wts<4>(lds, F_SL0 + (j >> 1), 4);
            w2run(28, 14, j * 14336, 2);
            wts<4>(lds, F_SL0 + 2 + (j >> 1), 4);
            w2run(42, 14, 57344 + j * 14336, 2);
            wtl(lds, F_HD, 4);
            unsigned xr[8]; v2u b2v[4];
            {
                const unsigned xo = (2 * hh) * 512 + (u & 7) * 16 + (2 * ((u >> 3) & 1) + (u >> 4)) * 4;
#pragma unroll
                for (int i = 0; i < 8; ++i) xr[i] = LD1(rw, xo + ((i >> 1) & 1) * 2048 + (i & 1) * 512, O_X1 + team2 * 131072 + (cb2 * 32 + dt * 16) * 512 + (2 * th + tq) * 128 + (i >> 2) * 4096);
#pragma unroll
                for (int i = 0; i < 4; ++i) b2v[i] = *(const v2u*)(a.b2 + cb2 * 64 + dt * 32 + i * 8 + hh * 4);
            }
            SB();
            _Pragma("unroll 1") for (unsigned n = 0; n < SPIN; ++n) {
                CFENCE();
                const v2u f = LD2(rw, ((lane & 3) * 4 + ((lane >> 2) & 1)) * 128, O_F3 + (team2 * 64 + j * 16 + th * 2) * 128);
                if (__all(NE2(f.x, f.y) == 0)) break;
                __builtin_amdgcn_s_sleep(1);
            }
            CFENCE();
            f16v c2[4];
#pragma unroll
            for (int i = 0; i < 4; ++i)
#pragma unroll
                for (int e = 0; e < 16; ++e) c2[i][e] = 0.f;
            float xb[16], gs[16];
            unsigned wr = j * 14336 + lane * 16;
            asm volatile("" : "+v"(wr));
            {
                const unsigned hb = O_H + team2 * 458752 + th * 229376 + j * 28672;
                constexpr int LA = 2 * (HWIN - 1), G0 = 28 - SK;
                v4u h[28][2];
                v4u wl[28][2];
                unsigned hos[28];
                v2u gq[4]; float r2 = 0.f;
                const unsigned hv0 = lane * 16 + tq * 114688, hv1 = lane * 16 + (tq ^ 1) * 114688;
                unsigned rr0 = ring + w2ra, rr1 = ring + (w2ra ^ 64), ra0 = j * 14336 + w2ra, ra1 = j * 14336 + (w2ra ^ 64), rb0 = ra0 + 57344, rb1 = ra1 + 57344;
                asm volatile("" : "+v"(rr0), "+v"(rr1), "+v"(ra0), "+v"(ra1), "+v"(rb0), "+v"(rb1));
                unsigned ho = hb + rk * 1024; const unsigned he = hb + 28672;
#define HG(n) ((n) < 2 * G0 ? (n) >> 1 : (n) < 2 * G0 + SK ? (n) - G0 : (n) - G0 - SK)
#define HI(n) ((n) < 2 * G0 ? (n) & 1 : (n) < 2 * G0 + SK ? 1 : 0)
#define HLN(n) do { if ((n) < 2 * G0 ? !((n) & 1) : (n) < 2 * G0 + SK) { hos[HG(n)] = ho; ho += 1024; if (ho == he) ho = hb; asm volatile("" : "+s"(ho)); } \
                    h[HG(n)][HI(n)] = LD4(rw, HI(n) ? hv1 : hv0, hos[HG(n)]); } while (0)
#define WSRC(g, i) ((g) < 14 ? ((i) ? rr1 : rr0) + ((g) - 9) * 2048 : (g) < 21 ? ((i) ? ra1 : ra0) + ((g) - 14) * 2048 : ((i) ? rb1 : rb0) + ((g) - 21) * 2048)
#define PUTQ(i, e) LF4(wr + ((i) - 1) * 4096 + (e) * 1024) = (f4){c2[i][4 * (e)], c2[i][4 * (e) + 1], c2[i][4 * (e) + 2], c2[i][4 * (e) + 3]}
#pragma unroll
                for (int n = 0; n < LA; ++n) { HLN(n); SB(); }
                sfor<56>([&](auto n_) __attribute__((always_inline)) {
                    constexpr int n = n_, g = HG(n), i = HI(n);
                    constexpr bool first = n < 2 * G0 ? i == 0 : n < 2 * G0 + SK;
                    const v4u a0 = 2 * g < NR ? wf[(2 * g) % NR] : wl[g][0], a1 = 2 * g < NR ? wf[(2 * g + 1) % NR] : wl[g][1];
                    c2[i] = mm32(a0, h[g][i], c2[i]);
                    SB();
                    if (n + LA < 56) HLN((n + LA) % 56);
                    if (first && 2 * (g + 1) >= NR && g + 1 < 28) wl[(g + 1) % 28][0] = trd2(lds, WSRC(g + 1, 0), WSRC(g + 1, 0) + 512);
                    if (n > 2 * G0 + SK && n - 2 * G0 - SK <= 4) { PUTQ(1, (n - 2 * G0 - SK - 1) & 3); }
                    SB();
                    c2[2 + i] = mm32(a1, h[g][i], c2[2 + i]);
                    SB();
                    if (first && 2 * (g + 1) >= NR && g + 1 < 28) wl[(g + 1) % 28][1] = trd2(lds, WSRC(g + 1, 1), WSRC(g + 1, 1) + 512);
                    if (n == 20) { r2 = LF1(L_R + ((2 * th + tq) * 32 + u) * 4); gq[0] = L2V(L_GB + (dt * 16 + hh * 2) * 4); gq[1] = L2V(L_GB + (dt * 16 + 4 + hh * 2) * 4); }
                    if (n == 21) { gq[2] = L2V(L_GB + (dt * 16 + 8 + hh * 2) * 4); gq[3] = L2V(L_GB + (dt * 16 + 12 + hh * 2) * 4); }
                    if (n >= 24 && n < 40) {
                        const int k = (n - 24) & 15; const unsigned gw = (k & 2) ? gq[k >> 2].y : gq[k >> 2].x, bw = (k & 2) ? b2v[k >> 2].y : b2v[k >> 2].x, xw = xr[k >> 1];
                        const float gv = (k & 1) ? bhi(gw) : blo(gw);
                        xb[k] = fm1(gv, (k & 1) ? bhi(bw) : blo(bw), (k & 1) ? bhi(xw) : blo(xw)); gs[k] = mu1(gv, r2);
                        asm volatile("" : "+v"(xb[k]), "+v"(gs[k]));
                    }
                    if (n > 2 * G0 + SK && n - 2 * G0 - SK <= 4) { PUTQ(3, (n - 2 * G0 - SK - 1) & 3); }
                    SB();
                });
            }
#pragma unroll
            for (int e = 0; e < 4; ++e) PUTQ(2, e);
            sign(lds, F_RD);
            const int tok = team2 * 128 + 16 * (u & 7) + 4 * (2 * th + tq) + 2 * ((u >> 3) & 1) + (u >> 4);
            unsigned op = (tok * 512 + cb2 * 64 + dt * 32 + hh * 8) * 2;
            const rsrc_t ro = mk(a.out, 2048 * 512 * 2);
            unsigned r1 = (j ^ 1) * 14336 + lane * 16, r2_ = (j ^ 2) * 14336 + 4096 + lane * 16, r3 = (j ^ 3) * 14336 + 8192 + lane * 16;
            asm volatile("" : "+v"(r1), "+v"(r2_), "+v"(r3), "+v"(op));
            wt(lds, F_RD, 4);
            f4 p[4][3];
#pragma unroll
            for (int e = 0; e < 4; ++e) { p[e][0] = LF4(r1 + e * 1024); p[e][1] = LF4(r2_ + e * 1024); p[e][2] = LF4(r3 + e * 1024); }
            SB();
            v2u o[4];
#pragma unroll
            for (int e = 0; e < 4; ++e) {
                f4 v = {c2[0][4 * e], c2[0][4 * e + 1], c2[0][4 * e + 2], c2[0][4 * e + 3]};
                v = (v + p[e][0]) + (p[e][1] + p[e][2]);
                v = __builtin_elementwise_fma(v, (f4){gs[4 * e], gs[4 * e + 1], gs[4 * e + 2], gs[4 * e + 3]}, (f4){xb[4 * e], xb[4 * e + 1], xb[4 * e + 2], xb[4 * e + 3]});
                o[e].x = pkb(v[0], v[1]); o[e].y = pkb(v[2], v[3]);
                if (e & 1) {
                    v2u rx_ = __builtin_amdgcn_permlane32_swap(o[e - 1].x, o[e].x, false, false);
                    v2u ry_ = __builtin_amdgcn_permlane32_swap(o[e - 1].y, o[e].y, false, false);
                    __builtin_amdgcn_raw_buffer_store_b128((v4u){rx_.x, ry_.x, rx_.y, ry_.y}, ro, op + (e - 1) * 16, 0, OAUX);
                }
            }
        }
        asm volatile(".Lkend:");
        return;
    }

    unsigned tag;
    {
        const unsigned tb_ = ch ? L_T1 : L_T0;
        if (lane < 16) L4(L_H + ch * H_CH + hf * 256 + lane * 16) = (v4u){0, 0, 0, 0};
        asm volatile("s_waitcnt vmcnt(4)" : "+v"(par[0]), "+v"(par[1]), "+v"(par[2]), "+v"(par[3]), "+v"(par[4]), "+v"(par[5]), "+v"(par[6]), "+v"(par[7]) :: "memory");
        const float dsk = (hf == 1 && lane == 63) ? blo(par[7]) : 0.f;
        const float dt = rbf(__builtin_amdgcn_exp2f(blo(par[6]) * 1.44269504f));
        const float lr2 = dt * blo(par[0]) * 1.44269504f, rev = dt * blo(par[1]) * 0.15915494f;
        const float br = dt * blo(par[2]), bi = dt * blo(par[3]), cr = blo(par[4]), ci = blo(par[5]);
        f2 W = {cr * br - ci * bi, cr * bi + ci * br};
        unsigned mx = max(asu(W.x) & 0x7fffffffu, asu(W.y) & 0x7fffffffu);
        mx = max(mx, asu(dppf<0x128>(asf(mx)))); mx = max(mx, asu(dppf<0x124>(asf(mx)))); mx = max(mx, asu(dppf<0x122>(asf(mx)))); mx = max(mx, asu(dppf<0x121>(asf(mx))));
        const unsigned mu = max(max((unsigned)__builtin_amdgcn_readlane(mx, 0), (unsigned)__builtin_amdgcn_readlane(mx, 16)), max((unsigned)__builtin_amdgcn_readlane(mx, 32), (unsigned)__builtin_amdgcn_readlane(mx, 48)));
        const unsigned ex = min(max(mu >> 23, 30u), 230u);
        const float sc = asf((266u - ex) << 23), isc = asf((ex - 12u) << 23);
        SETM0(j * XY_W);
#define XO(t, row) (L_XY + (t) * XY_T + (row) * 272)
#define PUT4(t, r0, dr, a0, a1, a2, a3) asm volatile("ds_write_addtid_b32 %0 offset:%4\n\tds_write_addtid_b32 %1 offset:%5\n\tds_write_addtid_b32 %2 offset:%6\n\tds_write_addtid_b32 %3 offset:%7" \
            :: "v"(a0), "v"(a1), "v"(a2), "v"(a3), "i"(XO(t, r0)), "i"(XO(t, (r0) + (dr))), "i"(XO(t, (r0) + 2 * (dr))), "i"(XO(t, (r0) + 3 * (dr))) : "memory")
        {
            const f2 y8 = cexpn(lr2, -rev, 8.f), y8p = perp(y8), y16 = cm(y8, y8, y8p), y32 = cm(y16, y16, perp(y16)), y32p = perp(y32);
            const f2 z1 = cexpn(lr2, rev, 1.f), z1p = perp(z1), z2 = cm(z1, z1, z1p), z2p = perp(z2), z4 = cm(z2, z2, z2p), z4p = perp(z4);
            const f2 zc = cexpn(lr2, rev, 128.f), zcp = perp(zc), zd = cm(zc, zc, zcp), zdp = perp(zd), ze = cm(zd, zd, zdp), zep = perp(ze), zk = cexpn(lr2, rev, 1024.f), zkp = perp(zk);
            f2 y[4] = {{1.f, 0.f}, y8, y16, cm(y16, y8, y8p)};
#pragma unroll
            for (int g = 0; g < 4; ++g) {
                const unsigned p0 = pkh(y[0]), p1 = pkh(y[1]), p2 = pkh(y[2]), p3 = pkh(y[3]);
                if (g < 3) {
#pragma unroll
                    for (int i = 0; i < 4; ++i) y[i] = (g == 0 && i == 0) ? y32 : cm(y[i], y32, y32p);
                    asm volatile("" : "+v"(y[0]), "+v"(y[1]), "+v"(y[2]), "+v"(y[3]));
                }
                PUT4(0, 4 * g, 1, p0, p1, p2, p3);
            }
            f2 X[8], X4[8], X2[4], X6[4];
            X[0] = W * sc; X[1] = cm(X[0], zc, zcp); X[2] = cm(X[0], zd, zdp); X[3] = cm(X[1], zd, zdp);
#pragma unroll
            for (int i = 0; i < 4; ++i) { X4[i] = cm(X[i], z4, z4p); X2[i] = cm(X[i], z2, z2p); }
            { const unsigned p0 = pkh(X[0]), p1 = pkh(X[1]), p2 = pkh(X[2]), p3 = pkh(X[3]), r0 = pkh(X4[0]), r1 = pkh(X4[1]), r2 = pkh(X4[2]), r3 = pkh(X4[3]), s0 = pkh(X2[0]), s1 = pkh(X2[1]), s2 = pkh(X2[2]), s3 = pkh(X2[3]);
#pragma unroll
              for (int i = 0; i < 4; ++i) { X6[i] = cm(X4[i], z2, z2p); X[4 + i] = cm(X[i], ze, zep); }
              asm volatile("" : "+v"(X6[0]), "+v"(X6[1]), "+v"(X6[2]), "+v"(X6[3]), "+v"(X[4]), "+v"(X[5]), "+v"(X[6]), "+v"(X[7]));
              PUT4(1, 0, 1, p0, p1, p2, p3); PUT4(2, 0, 1, r0, r1, r2, r3); PUT4(2, 8, 2, s0, s1, s2, s3); }
            { const unsigned p0 = pkh(X6[0]), p1 = pkh(X6[1]), p2 = pkh(X6[2]), p3 = pkh(X6[3]), r0 = pkh(X[4]), r1 = pkh(X[5]), r2 = pkh(X[6]), r3 = pkh(X[7]);
              f2 o[8];
#pragma unroll
              for (int i = 0; i < 2; ++i) { o[4 * i] = cm(X[i], z1, z1p); o[4 * i + 1] = cm(X2[i], z1, z1p); o[4 * i + 2] = cm(X4[i], z1, z1p); o[4 * i + 3] = cm(X6[i], z1, z1p); }
              asm volatile("" : "+v"(o[0]), "+v"(o[1]), "+v"(o[2]), "+v"(o[3]), "+v"(o[4]), "+v"(o[5]), "+v"(o[6]), "+v"(o[7]));
              PUT4(2, 9, 2, p0, p1, p2, p3); PUT4(1, 4, 1, r0, r1, r2, r3);
              const unsigned q0 = pkh(o[0]), q1 = pkh(o[1]), q2 = pkh(o[2]), q3 = pkh(o[3]), q4 = pkh(o[4]), q5 = pkh(o[5]), q6 = pkh(o[6]), q7 = pkh(o[7]);
#pragma unroll
              for (int i = 0; i < 4; ++i) X4[4 + i] = cm(X[4 + i], z4, z4p);
              X[0] = cm(X[0], zk, zkp); X2[0] = cm(X[0], zk, zkp);
              asm volatile("" : "+v"(X4[4]), "+v"(X4[5]), "+v"(X4[6]), "+v"(X4[7]), "+v"(X[0]), "+v"(X2[0]));
              PUT4(3, 0, 1, q0, q1, q2, q3); PUT4(3, 4, 1, q4, q5, q6, q7); }
            { const unsigned p0 = pkh(X4[4]), p1 = pkh(X4[5]), p2 = pkh(X4[6]), p3 = pkh(X4[7]), p4 = pkh(X2[0]);
#pragma unroll
              for (int i = 1; i < 4; ++i) X[i] = cm(X[i], zk, zkp);
              asm volatile("" : "+v"(X[1]), "+v"(X[2]), "+v"(X[3]));
              PUT4(2, 4, 1, p0, p1, p2, p3); WTID(p4, XO(3, 8)); }
            const unsigned fa = L_XY + j * XY_W + r16 * 272 + q * 16;
            v4u fr[4][4];
#pragma unroll
            for (int s = 0; s < 4; ++s) { fr[s][0] = L4(fa + s * 64); fr[s][2] = L4(fa + 2 * XY_T + s * 64); fr[s][3] = L4(fa + 3 * XY_T + s * 64); }
            SB();
            { const unsigned p0 = pkh(X[0]), p1 = pkh(X[1]), p2 = pkh(X[2]), p3 = pkh(X[3]);
#pragma unroll
              for (int i = 4; i < 8; ++i) X[i] = cm(X[i], zk, zkp);
              const unsigned r0 = pkh(X[4]), r1 = pkh(X[5]), r2 = pkh(X[6]), r3 = pkh(X[7]);
              PUT4(1, 8, 1, p0, p1, p2, p3); PUT4(1, 12, 1, r0, r1, r2, r3); }
#pragma unroll
            for (int s = 0; s < 4; ++s) fr[s][1] = L4(fa + XY_T + s * 64);
            SB();
            f4 d[3] = {{0, 0, 0, 0}, {0, 0, 0, 0}, {0, 0, 0, 0}};
#pragma unroll
            for (int s = 0; s < 4; ++s) { d[1] = mm16h(fr[s][2], fr[s][0], d[1]); d[2] = mm16h(fr[s][3], fr[s][0], d[2]); }
#pragma unroll
            for (int s = 0; s < 4; ++s) d[0] = mm16h(fr[s][1], fr[s][0], d[0]);
            if (lane == 63) d[2][3] = 0.f;
            const unsigned da = tb_ + hf * 3072 + lane * 16;
            LF4(da + 1024) = d[1] * isc; LF4(da + 2048) = d[2] * isc; LF4(da) = d[0] * isc;
        }
        sign(lds, F_KP0 + ch);
        asm volatile("s_waitcnt vmcnt(0)" : "+v"(b1v), "+v"(lt0), "+v"(lt1), "+v"(lt2) :: "memory");
        unsigned ta[19];
#pragma unroll
        for (int e = 0; e < 19; ++e) { const unsigned u_ = e < 8 ? lt0[(e >> 1) & 3] : e < 16 ? lt1[(e >> 1) & 3] : lt2[(e >> 1) & 1]; ta[e] = tb_ + ((e & 1) ? u_ >> 16 : u_ & 0xffff); asm volatile("" : "+v"(ta[e])); }
        wt(lds, F_KP0 + ch, 2);
        const unsigned kib = L_XY + ch * KI_CH;
        {
            float k0[19], k1[19], kv[19];
#pragma unroll
            for (int e = 0; e < 19; ++e) { k0[e] = LF1(ta[e]); k1[e] = LF1(ta[e] + 3072); }
            SB();
#pragma unroll
            for (int e = 0; e < 19; ++e) kv[e] = k0[e] + k1[e];
            kv[15] += dsk;
            const unsigned ka = kib + (hf * 64 + lane) * 32;
#pragma unroll
            for (int c = 0; c < 4; ++c)
#pragma unroll
                for (int hx = 0; hx < 2; ++hx) {
                    const int e = c + 8 * hx;
                    v4u pk_;
#pragma unroll
                    for (int i = 0; i < 4; ++i) { unsigned t_ = pkb(kv[e + 2 * i], kv[e + 2 * i + 1]); asm volatile("" : "+v"(t_)); pk_[i] = t_; }
                    L4(ka + c * KI_C + hx * 16) = pk_;
                }
            if (lane < 4) L4(kib + lane * KI_C + 4096 + hf * 16) = (v4u){0, 0, 0, 0};
        }
        sign(lds, F_KE0 + ch); wt(lds, F_KE0 + ch, 2);
        v4u af[4][8];
        const unsigned aa = kib + ((3 - r16) & 3) * KI_C + 2 * (2028 - 4 * (r16 >> 2) + 8 * q - 128 * hf - 32 * 59);
        v2u ah[2][8][2];
#pragma unroll
        for (int d = 0; d < 2; ++d)
#pragma unroll
            for (int t = 0; t < 8; ++t) { const unsigned o = aa + 64 * (59 - d - 8 * t);
                v2u p0 = L2V(o), p1 = L2V(o + 8); af[d][t] = (v4u){p0.x, p0.y, p1.x, p1.y}; }
        f4 acc[8];
#pragma unroll
        for (int c = 0; c < 8; ++c) { acc[c] = (f4){0, 0, 0, 0}; asm volatile("" : "+v"(acc[c])); }
        unsigned so = ((lane >> 3) & 1) * 131072 + q * 128 + (lane & 7) * 16 + O_Z + 4 * j * 131072 + b * 512;
        unsigned pa = L_EX + 2 * j * 1024 + lane * 16;
        asm volatile("" : "+v"(so), "+v"(pa));
        wt(lds, F_H, 4);
        __builtin_amdgcn_s_setprio(1);
        f4 xs[2][2];
#pragma unroll
        for (int c = 0; c < 8; ++c) asm volatile("" : "+v"(acc[c]));
        unsigned ea = L_EX + j * 8192 + lane * 16;
        {
            const unsigned ha = L_H + ch * H_CH + 2 * (240 + 16 * r16 + 8 * q - 128 * hf - 96);
            unsigned ba = L_EX + j * 8192 + (16 * r16 + 4 * q) * 4;
            asm volatile("" : "+v"(ba), "+v"(ea));
            v4u bf[2][8], xr[2];
#pragma unroll
            for (int e = 0; e < 8; ++e) { bf[0][e] = L4(ha + 64 * (8 * e + 3)); SB(); }
#pragma unroll
            for (int d = 0; d < 3; ++d) {
                int n = 0;
#pragma unroll
                for (int e = 0; e < 8; ++e)
#pragma unroll
                    for (int t = 0; t + e < 8; ++t) {
                        MMA(acc[t + e], af[d][t], bf[d & 1][e]);
                        SB();
                        if (n % 3 == 1 && n < 24) bf[(d + 1) & 1][n / 3] = L4(ha + 64 * (8 * (n / 3) + 3 - (d + 1)));
                        if (d == 0 && (n == 24 || n == 25)) xr[n & 1] = L4(ba + (n & 1) * 1024);
                        if (d == 0 && n < 32) ah[n >> 4][(n >> 1) & 7][n & 1] = L2V(aa + 64 * (57 - (n >> 4) - 8 * ((n >> 1) & 7)) + 8 * (n & 1));
                        if (d == 0 && n == 32) sign(lds, F_AF);
                        if (n == 35) __builtin_amdgcn_s_waitcnt(0xc07f);
                        if (d == 1 && n >= 24 && n < 32) { const int k = n - 24; unsigned u_ = xr[k >> 2][k & 3]; float lo = blo(u_), hi = bhi(u_); asm volatile("" : "+v"(lo), "+v"(hi)); xs[0][k >> 2][k & 3] = lo; xs[1][k >> 2][k & 3] = hi; }
                        ++n;
                        SB();
                    }
                if (d == 0) {
#pragma unroll
                    for (int k = 0; k < 16; ++k) af[2 + (k >> 3)][k & 7] = (v4u){ah[k >> 3][k & 7][0].x, ah[k >> 3][k & 7][0].y, ah[k >> 3][k & 7][1].x, ah[k >> 3][k & 7][1].y};
                }
            }
#pragma unroll
            for (int c = 0; c < 8; ++c) {
#pragma unroll
                for (int t = 0; t <= c; ++t) {
                    acc[c] = mm16(af[3][t], bf[1][c - t], acc[c]);
                    SB();
                    const int n = c * (c + 1) / 2 + t + 1;
                    const int k = n == 9 ? 0 : n == 13 ? 1 : n == 17 ? 2 : n == 21 ? 3 : n == 25 ? 4 : n == 30 ? 5 : n == 36 ? 6 : -1;
                    if (k >= 0) LF4(ea + k * 1024) = acc[k];
                    SB();
                }
            }
            LF4(ea + 7 * 1024) = acc[7];
        }
        sign(lds, F_EX);
        wt(lds, F_EX, 5);
        {
            const v4u mv = L4(L_MV);
            f4 p[2][4];
#pragma unroll
            for (int t = 0; t < 2; ++t)
#pragma unroll
                for (int i = 0; i < 4; ++i) p[t][i] = LF4(pa + i * 8192 + t * 1024);
            sign(lds, F_ZD);
            SB();
            tag = mv.z;
            const float ga0 = blo(mv.x), ga1 = bhi(mv.x), c10 = blo(mv.y), c11 = bhi(mv.y);
#pragma unroll
            for (int t = 0; t < 2; ++t) {
                f4 u0 = __builtin_elementwise_fma(p[t][0] + p[t][1], (f4){ga0, ga0, ga0, ga0}, xs[0][t]), u1 = __builtin_elementwise_fma(p[t][2] + p[t][3], (f4){ga1, ga1, ga1, ga1}, xs[1][t]);
                asm volatile("" : "+v"(u0), "+v"(u1));
                f4 s0 = u0 * c10, s1 = u1 * c11;
                asm volatile("" : "+v"(s0), "+v"(s1));
                v4u xo, zo;
#pragma unroll
                for (int e = 0; e < 4; ++e) { xo[e] = pkb(u0[e], u1[e]); zo[e] = pkb(s0[e], s1[e]); }
                ST4(zo, rw, so, 2 * t * 131072);
                ST4(xo, rw, so, O_X1 - O_Z + 2 * t * 131072);
            }
        }
        __builtin_amdgcn_s_setprio(0);
        VM(0);
        if (lane < 32) ST1(tag, rw, lane * 4, O_F2 + j * 32768 + b * 128);
    }
    {
        unsigned apa = L_AP + r16 * 8, rra = L_R + (j * 32 + (lane & 31)) * 4;
        v4u p8[2];
        v2u ap[7], bA, bB; float r2v; unsigned bad, stf = 0;
        float f0, f1; unsigned u0, u1;
#define PIN2(a, b) asm volatile("" : "+v"(a), "+v"(b))
        auto fin = [&](int s) __attribute__((always_inline)) {
            if (s == 0) { f0 = asf(p8[0].x) + asf(p8[1].x); asm volatile("" : "+v"(f0)); f1 = asf(p8[0].z) + asf(p8[1].z); PIN2(f0, f1); }
            if (s == 1) { bad = NE2(p8[0].y, p8[0].w) | NE2(p8[1].y, p8[1].w); asm volatile("" : "+v"(bad)); }
            if (s == 2) { const v2u r = __builtin_amdgcn_permlane32_swap(asu(f0), asu(f1), false, false); u0 = r.x; u1 = r.y; f0 = asf(u0) + asf(u1); asm volatile("" : "+v"(f0)); }
            if (s == 3) { f0 *= (1.f / 512.f); f1 = f0; PIN2(f0, f1); }
            if (s == 4) { const v2u r = __builtin_amdgcn_permlane32_swap(asu(f0), asu(f1), false, false); u0 = r.x; u1 = r.y; f0 = asf(u0); f1 = __builtin_fmaf(-f0, f0, asf(u1)); PIN2(f0, f1); }
            if (s == 5) { f1 = rbf(asf(max((int)asu(f1), 0))); asm volatile("" : "+v"(f1)); }
            if (s == 6) { f1 = rbf(f1 + 1e-5f); asm volatile("" : "+v"(f1)); }
            if (s == 7) { f1 = __builtin_amdgcn_sqrtf(f1); u0 = pkb(f0, f0) ^ 0x80008000u; PIN2(f1, u0); }
            if (s == 8) { u1 = pkb(f1, f1); f1 = __builtin_amdgcn_rcpf(blo(u1)); PIN2(f1, u1); }
            if (s == 9) { bA = q == 0 ? (v2u){u0, u1} : (v2u){0, 0}; bB = q == 1 ? (v2u){u0, u1} : (v2u){0, 0}; r2v = f1 * f1; asm volatile("" : "+v"(bA), "+v"(bB), "+v"(r2v)); }
        };
        unsigned hs0 = O_H + team1 * 458752 + j * 114688 + mem1 * 7168 + lane * 16, hs1 = hs0 + 4096;
        const bool early = rfl(*(volatile LAS unsigned*)(lds + L_FLG + F_W1 * 4)) >= 4;
        if (early) { st_job(b1v, j); sign(lds, F_ST); } else __builtin_amdgcn_s_sleep(P2L);
        f4 c1[14];
#pragma unroll
        for (int i = 0; i < 14; ++i) { c1[i] = (f4){0, 0, 0, 0}; asm volatile("" : "+v"(c1[i])); }
        {
            const unsigned zo = (lane >> 3) * 512 + (lane & 7) * 16, zo2 = zo + 4096, zb = O_Z + team1 * 131072 + j * 128;
            unsigned wa = (4 * q + (r16 >> 2)) * 224 + (r16 & 3) * 8;
            v4u z[16][2];
            v2u wv[2][14];
#define ZL1(sl, i, so) z[sl][i] = LD4(rw, (i) ? zo2 : zo, so)
#define XC1(d, a, b) asm volatile("v_cndmask_b32_dpp %0, %1, %2, vcc row_ror:8 row_mask:0xf bank_mask:0xf" : "=&v"(d) : "v"(a), "v"(b) : "vcc")
#define XCM(m) asm volatile("s_mov_b64 vcc, %0" :: "s"(m) : "vcc")
            const unsigned long long mlo = 0x00ff00ff00ff00ffull, mhi = ~mlo;
#define XPART(k, zs, A, B) do { unsigned x_; const v4u zz = zs[((k) >> 1) & 1]; \
                \
                if ((k) < 4) { if ((k) & 1) XC1(x_, zz.w, zz.y); else XC1(x_, zz.z, zz.x); } else { if ((k) & 1) XC1(x_, zz.y, zz.w); else XC1(x_, zz.x, zz.z); } \
                if ((k) & 1) B[((k) & 2) + ((k) >> 2)] = x_; else A[((k) & 2) + ((k) >> 2)] = x_; } while (0)
            unsigned gz[4];
#pragma unroll
            for (int g = 0; g < 4; ++g) gz[g] = zb + ((rot + g) & 3) * 32768;
#define ZLN(n) ZL1((n) >> 1, (n) & 1, gz[(n) >> 3] + (((n) >> 1) & 3) * 8192)
            unsigned wb = wa + 57344;
            asm volatile("" : "+v"(wa), "+v"(wb));
        {
            bool ok = false;
            asm volatile("" : "+v"(apa), "+v"(rra), "+v"(hs0), "+v"(hs1));
            if (P2A) __builtin_amdgcn_s_sleep(P2A);
            _Pragma("unroll 1") for (unsigned n = 0; n < SPIN; ++n) {
                CFENCE();
                if (!ok) { const v2u f = LD2(rw, lane * 128, O_F2 + (team1 >> 2) * 32768 + j * 8192); ok = NE2(f.x, f.y) == 0; }
                if (__all(ok)) break;
                __builtin_amdgcn_s_sleep(1);
            }
            CFENCE();
            sign(lds, F_E2);
            wt(lds, F_E2, 4);
        }
#pragma unroll
            for (int n = 0; n < ZPRE; ++n) { ZLN(n); SB(); }
            if (!early) { st_job(b1v, j); sign(lds, F_ST); }
#pragma unroll
            for (int t = 0; t < 14; ++t) wv[0][t] = trd(lds, wa + (t >> 1) * 32 + (t & 1) * 3584);
            SB();
            __builtin_amdgcn_s_setprio(1);
            v4u fa, fb;
#pragma unroll
            for (int k = 0; k < 8; ++k) { if (k == 0) XCM(mlo); if (k == 4) XCM(mhi); XPART(k, z[0], fa, fb); }
            __builtin_amdgcn_s_waitcnt(0xc07f);
            asm volatile("s_nop 1" : "+v"(fa), "+v"(fb));
#pragma unroll
            for (int ks = 0; ks < 15; ++ks) {
                v4u na, nb;
#pragma unroll
                for (int t = 0; t < 14; ++t) {
                    const v2u w0 = wv[ks & 1][t & ~1], w1_ = wv[ks & 1][t | 1];
                    MMA(c1[t], ((v4u){w0.x, w0.y, w1_.x, w1_.y}), (t & 1) ? fb : fa);
                    SB();
                    if (ks < 15) {
                        wv[(ks + 1) & 1][t] = trd(lds, (ks + 1 < 8 ? wa : wb) + ((ks + 1) & 7) * 7168 + (t >> 1) * 32 + (t & 1) * 3584);
                        SB();
                        if (t == 3) { XCM(mlo); __builtin_amdgcn_s_waitcnt(0xc87f); }
                        if (t >= 4 && t < 8) XPART(t - 4, z[ks + 1], na, nb);
                        if (t == 8) { XCM(mhi); __builtin_amdgcn_s_waitcnt(0xc97f); }
                        if (t >= 9 && t < 13) XPART(t - 5, z[ks + 1], na, nb);
                        if (t == 13) __builtin_amdgcn_s_waitcnt(0xca7f);
                    }
                    if (t < ZPER && ZPRE + ks * ZPER + t < 32) ZLN((ZPRE + ks * ZPER + t) & 31);
                    if ((ks & 3) == 3 && t == 2) sign(lds, F_SL0 + (ks >> 2));
                    if (ks == 14 && t == 0) stf = L1(L_FLG + F_ST * 4);
                    if (ks == PSK && t < 2) p8[t] = LD4(rw, lane * 16 + t * 1024, O_PS + team1 * 8192 + j * 2048);
                    SB();
                }
                fa = na; fb = nb;
            }
            if (__builtin_expect(rfl(stf) < 8, 0)) wt(lds, F_ST, 8);
            sign(lds, F_SL0 + 3);
#pragma unroll
            for (int t = 0; t < 14; ++t) {
                const v2u w0 = wv[1][t & ~1], w1_ = wv[1][t | 1];
                MMA(c1[t], ((v4u){w0.x, w0.y, w1_.x, w1_.y}), (t & 1) ? fb : fa);
                SB();
                if (t < 4) { ap[2 * t] = L2V(apa + 2 * t * 128); if (t < 3) ap[2 * t + 1] = L2V(apa + (2 * t + 1) * 128); }
                if (t >= 3 && t < 13) fin(t - 3);
                if (t == 13) LF1(rra) = r2v;
                SB();
            }
        }
        {
            if (__builtin_expect(!__all(bad == 0), 0)) {
                _Pragma("unroll 1") for (unsigned n = 0; n < SPIN; ++n) {
                    CFENCE();
#pragma unroll
                    for (int i = 0; i < 2; ++i) p8[i] = LD4(rw, lane * 16 + i * 1024, O_PS + team1 * 8192 + j * 2048);
                    if (__all((NE2(p8[0].y, p8[0].w) | NE2(p8[1].y, p8[1].w)) == 0)) break;
                }
#pragma unroll
                for (int s = 0; s < 10; ++s) fin(s);
                LF1(rra) = r2v;
            }
            SB();
#pragma unroll
            for (int t = 0; t < 4; ++t) c1[t] = mm16s(ap[t >> 1], (t & 1) ? bB : bA, c1[t]);
            SB();
#pragma unroll
            for (int t = 0; t < 7; ++t) {
                f4 uA = c1[2 * t], uB = c1[2 * t + 1];
                if (t < 5) { c1[2 * t + 4] = mm16s(ap[t + 2], bA, c1[2 * t + 4]); SB(); }
#pragma unroll
                for (int e = 0; e < 4; ++e) { const float ta_ = uA[e]; uA[e] = asf(max((int)asu(ta_), 0)); }
                asm volatile("" : "+v"(uA));
                SB();
                if (t < 5) { c1[2 * t + 5] = mm16s(ap[t + 2], bB, c1[2 * t + 5]); SB(); }
#pragma unroll
                for (int e = 0; e < 4; ++e) { const float tb_ = uB[e]; uB[e] = asf(max((int)asu(tb_), 0)); }
                asm volatile("" : "+v"(uB));
                SB();
                uA *= uA; uB *= uB;
                v2u s0 = __builtin_amdgcn_permlane16_swap(pkb(uA[0], uA[1]), pkb(uB[0], uB[1]), false, false);
                v2u s1 = __builtin_amdgcn_permlane16_swap(pkb(uA[2], uA[3]), pkb(uB[2], uB[3]), false, false);
                ST4(((v4u){s0.x, s1.x, s0.y, s1.y}), rw, (t < 4 ? hs0 : hs1), (t & 3) * 1024);
                SB();
            }
        }
        VM(0);
        if (lane < 32) ST1(tag, rw, lane * 4, O_F3 + ((team1 * 16 + mem1) * 4 + j) * 128);
        sign(lds, F_HD);
    }
}

extern "C" void kernel_launch(void* const* d_in, const int* in_sizes, int n_in, void* d_out, int out_size, void* d_ws, size_t ws_size, hipStream_t stream) {
    Args a;
    a.x = (const u16*)d_in[0]; a.Are = (const u16*)d_in[1]; a.Aim = (const u16*)d_in[2]; a.Bre = (const u16*)d_in[3]; a.Bim = (const u16*)d_in[4];
    a.Cre = (const u16*)d_in[5]; a.Cim = (const u16*)d_in[6]; a.ldt = (const u16*)d_in[7]; a.Dsk = (const u16*)d_in[8];
    a.w1 = (const u16*)d_in[10]; a.b1 = (const u16*)d_in[11]; a.w2 = (const u16*)d_in[12]; a.b2 = (const u16*)d_in[13];
    a.c = (const u16*)d_in[14]; a.wm = (const u16*)d_in[15];
    a.out = (u16*)d_out; a.ws = (char*)d_ws;
    (void)hipFuncSetAttribute((const void*)fused, hipFuncAttributeMaxDynamicSharedMemorySize, LDS_BYTES);
    fused<<<256, 512, LDS_BYTES, stream>>>(a);
}
```
